```python
import torch

def _softmax(x, axis=-1):
    return torch.softmax(x, dim=axis)
def _gelu(x):
    return torch.nn.functional.gelu(x, approximate="tanh")
def _silu(x):
    return torch.nn.functional.silu(x)
def _sigmoid(x):
    return torch.sigmoid(x)
def _layernorm(x, axis=-1, eps=1e-5):
    m = x.mean(dim=axis, keepdim=True)
    v = x.var(dim=axis, keepdim=True, unbiased=False)
    return (x - m) / torch.sqrt(v + eps)
def _scatter_add(tgt, idx, src, axis=0):
    return tgt.clone().index_add_(axis, idx, src)
def _segment_sum(src, seg, n):
    out = torch.zeros((n,) + src.shape[1:], dtype=src.dtype)
    return out.index_add_(0, seg, src)
def _pad(x, pw):
    flat = tuple(v for pair in reversed(pw) for v in pair)
    return torch.nn.functional.pad(x, flat)

kernel_name = 'interleave_layers__softmax_full_mha__dense_activation_mish__prompt'

def setup_inputs():

    _w = (lambda a: (lambda t: t.to(torch.bfloat16) if t.is_floating_point() else t)(torch.as_tensor(a)))
    def _A_setup():
        B, S, D = 1, 2048, 512
        return {
            "x": _w((lambda *s: torch.randn(*s, dtype=torch.bfloat16) * 0.05)(B, S, D)),
            "wqkv": _w((lambda *s: torch.randn(*s, dtype=torch.bfloat16) * 0.05)(D, 3 * D)),
            "wo": _w((lambda *s: torch.randn(*s, dtype=torch.bfloat16) * 0.05)(D, D)),
        }
    _A_in = _A_setup()

    def _B_setup():
        B, S, D, F = 1, 2048, 512, 1792
        return {
            "x": _w((lambda *s: torch.randn(*s, dtype=torch.bfloat16) * 0.05)(B, S, D)),
            "w1": _w((lambda *s: torch.randn(*s, dtype=torch.bfloat16) * 1.6)(D, F) * (D ** -0.5)),
            "b1": _w((lambda *s: torch.randn(*s, dtype=torch.bfloat16) * 0.4)(F)),
            "w2": _w((lambda *s: torch.randn(*s, dtype=torch.bfloat16) * 0.2)(F, D) * (F ** -0.5)),
        }
    _B_in = _B_setup()

    return {
        "A_x": _A_in["x"],
        "A_wqkv": _A_in["wqkv"],
        "A_wo": _A_in["wo"],
        "B_x": _B_in["x"],
        "B_w1": _B_in["w1"],
        "B_b1": _B_in["b1"],
        "B_w2": _B_in["w2"],
    }

def reference(A_x, A_wqkv, A_wo, B_x, B_w1, B_b1, B_w2):
    def _A_ref(x, wqkv, wo):
        B, S, D = x.shape
        H = 32; Dh = D // H
        qkv = (lambda x, *d: x.permute(*d))((x @ wqkv).reshape(B, S, 3, H, Dh), 2, 0, 3, 1, 4)
        q, k, v = qkv[0], qkv[1], qkv[2]
        scale = Dh ** -0.5
        scores = torch.einsum("bhqd,bhkd->bhqk", q, k) * scale
        p = _softmax(scores, axis=-1)
        o = torch.einsum("bhqk,bhkd->bhqd", p, v)
        return (lambda x, *d: x.permute(*d))(o, 0, 2, 1, 3).reshape(B, S, D) @ wo

    def _B_ref(x, w1, b1, w2):
        h = torch.einsum("bsd,df->bsf", x, w1) + b1
        sp = torch.log1p(torch.exp(-torch.abs(h))) + torch.where(h > 0, h, torch.zeros_like(h))
        act = h * torch.tanh(sp)
        return torch.einsum("bsf,fd->bsd", act, w2)

    x = A_x
    x = x + _A_ref(_layernorm(x, axis=-1), A_wqkv, A_wo)
    x = x + _B_ref(_layernorm(x, axis=-1), B_w1, B_b1, B_w2)
    x = x + _A_ref(_layernorm(x, axis=-1), A_wqkv, A_wo)
    x = x + _B_ref(_layernorm(x, axis=-1), B_w1, B_b1, B_w2)
    return x
```

```cpp
#include <hip/hip_runtime.h>
#include <hip/hip_bf16.h>
#include <stdint.h>

typedef short bf16x8 __attribute__((ext_vector_type(8)));
typedef short s16x4 __attribute__((ext_vector_type(4)));
typedef __bf16 bf16x2 __attribute__((ext_vector_type(2)));
typedef float f32x4 __attribute__((ext_vector_type(4)));
typedef float f32x16 __attribute__((ext_vector_type(16)));
typedef unsigned u32x2 __attribute__((ext_vector_type(2)));
typedef unsigned u32x4 __attribute__((ext_vector_type(4)));
typedef unsigned short u16;
#define LDSP(T) __attribute__((address_space(3))) T*
#define DI __device__ __forceinline__

constexpr int SEQ = 2048, DM = 512, NH = 32, DH = 16, FF = 1792;

DI unsigned cvtpk(float lo, float hi) { unsigned r; asm("v_cvt_pk_bf16_f32 %0, %1, %2" : "=v"(r) : "v"(lo), "v"(hi)); return r; }
DI float bflo(unsigned u) { return __uint_as_float(u << 16); }
DI float bfhi(unsigned u) { return __uint_as_float(u & 0xffff0000u); }
DI float dot2(unsigned a, unsigned b, float c) {
  return __builtin_amdgcn_fdot2_f32_bf16(__builtin_bit_cast(bf16x2, a), __builtin_bit_cast(bf16x2, b), c, false);
}
DI void glds16(const void* sbase, unsigned voff, unsigned lds_addr) {
  unsigned sv;
  asm volatile("s_mov_b32 %0, m0\n\ts_mov_b32 m0, %3\n\ts_nop 0\n\tglobal_load_lds_dwordx4 %1, %2\n\ts_mov_b32 m0, %0"
               : "=&s"(sv) : "v"(voff), "s"(sbase), "s"(lds_addr) : "memory");
}
template <int N> DI void wait_bar() { asm volatile("s_waitcnt vmcnt(%0) lgkmcnt(0)\n\ts_barrier" ::"i"(N) : "memory"); }
template <int N> DI void wait_vm() { asm volatile("s_waitcnt vmcnt(%0)" ::"i"(N) : "memory"); }
DI void lds_bar() { asm volatile("s_waitcnt lgkmcnt(0)\n\ts_barrier" ::: "memory"); }
DI s16x4 trread(unsigned addr) {
  return __builtin_bit_cast(s16x4, __builtin_amdgcn_ds_read_tr16_b64_v4i16((LDSP(s16x4))(size_t)addr));
}
DI bf16x8 ldsread16(unsigned addr) { return *(const LDSP(bf16x8))(size_t)addr; }
DI float xor16(float v) { return __shfl_xor(v, 16, 64); }
DI float xor32(float v) { return __shfl_xor(v, 32, 64); }

enum { EPI_QKV = 0, EPI_FFN1 = 1, EPI_RES = 2 };

struct GemmArgs {
  const u16* A;
  const u16* B;
  const u16* aux;
  u16* out0;
  u16* out1;
  u16* out2;
  float* kmax;
};

template <int BN> struct BL {
  static constexpr bool PADDED = (BN != 64);
  static constexpr int RB = BN * 2;
  static constexpr int GS = (BN == 112) ? 960 : (BN == 96 ? 784 : 512);
  static constexpr int STAGE = 16 * GS;
  static constexpr int NI = PADDED ? 16 : 8;
};

template <int BM, int BN, int KTOT, int N, int WM, int WN, int NS, int EPI, int MAP>
__global__ __launch_bounds__(512, 2) void gemm_k(GemmArgs p) {
  constexpr int NK = KTOT / 64;
  constexpr int ASTAGE = BM * 128;
  constexpr int BSTAGE = BL<BN>::STAGE;
  constexpr int STAGE = ASTAGE + BSTAGE;
  constexpr int MF = BM / WM / 16, NF = BN / WN / 16;
  static_assert(MF == 2, "MF must be 2");
  static_assert(WM * WN == 4, "8 waves");
  constexpr int LA = BM / 64;
  constexpr int LB = BL<BN>::NI / 8;
  constexpr int L = LA + LB;
  constexpr bool LN = (EPI != EPI_RES);
  constexpr int XBYTES = 8 * NF * 1024 + 8 * 32 * 8 + 2 * BN * 4 * 4;
  constexpr int SMEM = (NS * STAGE > XBYTES) ? NS * STAGE : XBYTES;
  __shared__ __attribute__((aligned(256))) char smem[SMEM];
  const unsigned lds0 = (unsigned)(size_t)(LDSP(char))smem;

  const int tid = threadIdx.x, lane = tid & 63;
  const int wid = __builtin_amdgcn_readfirstlane(tid >> 6);
  const int kh = wid >> 2, wmn = wid & 3, wm = wmn / WN, wn = wmn % WN;
  const int l15 = lane & 15, g = lane >> 4;

  constexpr int GM = SEQ / BM, GN = N / BN;
  int pm, pn;
  {
    const int bid = blockIdx.x, xcd = bid & 7, idx = bid >> 3;
    if (MAP == 0) {
      constexpr int PER = GM / 8;
      pm = xcd * PER + idx / GN; pn = idx % GN;
    } else {
      constexpr int PM = GM / 4, PN = GN / 2;
      pm = (xcd >> 1) * PM + idx / PN; pn = (xcd & 1) * PN + idx % PN;
    }
  }
  const int m0 = pm * BM, n0 = pn * BN;

  unsigned aoff[LA], boff[LB];
#pragma unroll
  for (int i = 0; i < LA; ++i) {
    const int ins = wid * LA + i;
    const int row = ins * 8 + (lane >> 3), ch = (lane & 7) ^ ((row >> 1) & 7);
    aoff[i] = (unsigned)(row * KTOT * 2 + ch * 16);
  }
#pragma unroll
  for (int i = 0; i < LB; ++i) {
    const int ins = wid * LB + i;
    if (BL<BN>::PADDED) {
      constexpr int CPR = BN / 8;
      const int row = ins * 4 + lane / CPR, ch = lane % CPR;
      boff[i] = (unsigned)(row * N * 2 + ch * 16);
    } else {
      const int row = ins * 8 + (lane >> 3);
      const int sw = ((row >> 1) & 1) | (((row >> 3) & 1) << 1);
      const int ch = (lane & 7) ^ (sw << 1);
      boff[i] = (unsigned)(row * N * 2 + ch * 16);
    }
  }
  const char* abase = (const char*)p.A + (size_t)m0 * KTOT * 2;
  const char* bbase = (const char*)p.B + (size_t)n0 * 2;

  auto issue = [&](int s) {
    const unsigned slot = lds0 + (s % NS) * STAGE;
#pragma unroll
    for (int i = 0; i < LA; ++i) glds16(abase + s * 128, aoff[i], slot + (wid * LA + i) * 1024);
    if (BL<BN>::PADDED) {
      if (lane < 4 * (BN / 8)) {
#pragma unroll
        for (int i = 0; i < LB; ++i) glds16(bbase + (size_t)s * 64 * N * 2, boff[i], slot + ASTAGE + (wid * LB + i) * BL<BN>::GS);
      }
    } else {
#pragma unroll
      for (int i = 0; i < LB; ++i) glds16(bbase + (size_t)s * 64 * N * 2, boff[i], slot + ASTAGE + (wid * LB + i) * 1024);
    }
  };

#pragma unroll
  for (int s = 0; s < NS - 1; ++s) issue(s);

  const unsigned a_rd = lds0 + (wm * 32 + l15) * 128 + (((kh * 4 + g) ^ (l15 >> 1)) * 16);
  unsigned b_rd[(BN == 64) ? NF : 1];
  {
    const int q = l15 >> 2, pp = lane & 3;
    const int row = kh * 32 + 8 * g + q;
    if (BL<BN>::PADDED) {
      b_rd[0] = lds0 + ASTAGE + (row >> 2) * BL<BN>::GS + (row & 3) * BL<BN>::RB + wn * NF * 32 + pp * 8;
    } else {
      const int sw = ((row >> 1) & 1) | (((row >> 3) & 1) << 1);
#pragma unroll
      for (int nf = 0; nf < NF; ++nf) b_rd[nf] = lds0 + ASTAGE + row * 128 + (((wn * NF + nf) ^ sw) * 32) + pp * 8;
    }
  }

  f32x4 acc[MF][NF];
#pragma unroll
  for (int a = 0; a < MF; ++a)
#pragma unroll
    for (int b = 0; b < NF; ++b) acc[a][b] = f32x4{0.f, 0.f, 0.f, 0.f};
  float rs[MF] = {0.f, 0.f}, rq[MF] = {0.f, 0.f};
  constexpr int NCS = (NF + 3) / 4;
  float cs[NCS];
#pragma unroll
  for (int i = 0; i < NCS; ++i) cs[i] = 0.f;
  const unsigned ONES = 0x3f803f80u;

#pragma unroll
  for (int s = 0; s < NK; ++s) {
    constexpr int dummy = 0; (void)dummy;
    const int ahead = (NS - 2 < NK - 1 - s) ? NS - 2 : NK - 1 - s;
    switch (ahead) {
      case 0: wait_bar<0>(); break;
      case 1: wait_bar<L>(); break;
      case 2: wait_bar<2 * L>(); break;
      case 3: wait_bar<3 * L>(); break;
      case 4: wait_bar<4 * L>(); break;
      case 5: wait_bar<5 * L>(); break;
      case 6: wait_bar<6 * L>(); break;
      default: wait_bar<7 * L>(); break;
    }
    if (s + NS - 1 < NK) issue(s + NS - 1);
    const unsigned so = (s % NS) * STAGE;
    bf16x8 af[MF];
#pragma unroll
    for (int mf = 0; mf < MF; ++mf) af[mf] = ldsread16(a_rd + so + mf * 2048);
#pragma unroll
    for (int nf = 0; nf < NF; ++nf) {
      s16x4 lo, hi;
      if (BL<BN>::PADDED) {
        lo = trread(b_rd[0] + so + nf * 32);
        hi = trread(b_rd[0] + so + nf * 32 + BL<BN>::GS);
      } else {
        lo = trread(b_rd[nf] + so);
        hi = trread(b_rd[nf] + so + 512);
      }
      const bf16x8 bfr = __builtin_shufflevector(lo, hi, 0, 1, 2, 3, 4, 5, 6, 7);
#pragma unroll
      for (int mf = 0; mf < MF; ++mf) acc[mf][nf] = __builtin_amdgcn_mfma_f32_16x16x32_bf16(bfr, af[mf], acc[mf][nf], 0, 0, 0);
      if (LN) {
        if ((nf & 3) == wm) {
          const u32x4 u = __builtin_bit_cast(u32x4, bfr);
          float c = cs[nf >> 2];
          c = dot2(u[0], ONES, c); c = dot2(u[1], ONES, c); c = dot2(u[2], ONES, c); c = dot2(u[3], ONES, c);
          cs[nf >> 2] = c;
        }
      }
    }
    if (LN) {
#pragma unroll
      for (int mf = 0; mf < MF; ++mf) {
        const u32x4 u = __builtin_bit_cast(u32x4, af[mf]);
#pragma unroll
        for (int j = 0; j < 4; ++j) { rs[mf] = dot2(u[j], ONES, rs[mf]); rq[mf] = dot2(u[j], u[j], rq[mf]); }
      }
    }
  }

  lds_bar();
  char* xb = smem;
  float* st = (float*)(smem + 8 * NF * 1024);
  float* csb = (float*)(smem + 8 * NF * 1024 + 8 * 32 * 8);
#pragma unroll
  for (int nf = 0; nf < NF; ++nf) *(f32x4*)(xb + (wid * NF + nf) * 1024 + lane * 16) = (kh == 0) ? acc[1][nf] : acc[0][nf];
  if (LN) {
#pragma unroll
    for (int mf = 0; mf < MF; ++mf) {
      float a = rs[mf], b = rq[mf];
      a += xor16(a); b += xor16(b); a += xor32(a); b += xor32(b);
      if (g == 0) { st[(wid * 32 + mf * 16 + l15) * 2] = a; st[(wid * 32 + mf * 16 + l15) * 2 + 1] = b; }
    }
#pragma unroll
    for (int i = 0; i < NCS; ++i) {
      const int nf = i * 4 + wm;
      float c = cs[i];
      c += xor16(c); c += xor32(c);
      if (nf < NF && g == 0) csb[kh * BN + nf * 16 + l15] = c;
    }
  }
  lds_bar();
  f32x4 r[NF];
#pragma unroll
  for (int nf = 0; nf < NF; ++nf) {
    const f32x4 o = *(const f32x4*)(xb + ((wid ^ 4) * NF + nf) * 1024 + lane * 16);
    r[nf] = ((kh == 0) ? acc[0][nf] : acc[1][nf]) + o;
  }
  const int trow = m0 + wm * 32 + kh * 16 + l15;
  if (LN) {
    const int ri = kh * 16 + l15;
    const float s1 = st[(wmn * 32 + ri) * 2] + st[((wmn + 4) * 32 + ri) * 2];
    const float s2 = st[(wmn * 32 + ri) * 2 + 1] + st[((wmn + 4) * 32 + ri) * 2 + 1];
    const float mean = s1 * (1.f / KTOT);
    const float var = fmaxf(s2 * (1.f / KTOT) - mean * mean, 0.f);
    const float rstd = 1.0f / sqrtf(var + 1e-5f);
    const float nmr = -mean * rstd;
#pragma unroll
    for (int nf = 0; nf < NF; ++nf) {
      const f32x4 c0 = *(const f32x4*)(csb + nf * 16 + g * 4), c1 = *(const f32x4*)(csb + BN + nf * 16 + g * 4);
      const int col = n0 + nf * 16 + g * 4;
      if (EPI == EPI_QKV) {
        float y[4];
#pragma unroll
        for (int j = 0; j < 4; ++j) y[j] = r[nf][j] * rstd + nmr * (c0[j] + c1[j]);
        const int colf = n0 + nf * 16, which = colf >> 9, head = (colf & 511) >> 4;
        if (which == 0) {
          const float sc = 0.25f * 1.4426950408889634f;
          u32x2 o = {cvtpk(y[0] * sc, y[1] * sc), cvtpk(y[2] * sc, y[3] * sc)};
          *(u32x2*)(p.out0 + ((size_t)head * SEQ + trow) * 16 + g * 4) = o;
        } else if (which == 1) {
          u32x2 o = {cvtpk(y[0], y[1]), cvtpk(y[2], y[3])};
          *(u32x2*)(p.out1 + (size_t)head * SEQ * 16 + (trow >> 5) * 512 + (g >> 1) * 256 + (trow & 31) * 8 + (g & 1) * 4) = o;
          float n2 = y[0] * y[0] + y[1] * y[1] + y[2] * y[2] + y[3] * y[3];
          n2 += xor16(n2); n2 += xor32(n2);
          n2 = fmaxf(n2, __shfl_xor(n2, 1, 64)); n2 = fmaxf(n2, __shfl_xor(n2, 2, 64));
          n2 = fmaxf(n2, __shfl_xor(n2, 4, 64)); n2 = fmaxf(n2, __shfl_xor(n2, 8, 64));
          if (lane == 0) p.kmax[head * 128 + (trow >> 4)] = n2;
        } else {
          u32x2 o = {cvtpk(y[0], y[1]), cvtpk(y[2], y[3])};
          *(u32x2*)(p.out2 + ((size_t)head * SEQ + trow) * 16 + g * 4) = o;
        }
      } else {
        const u32x2 bb = *(const u32x2*)(p.aux + col);
        const float bias[4] = {bflo(bb[0]), bfhi(bb[0]), bflo(bb[1]), bfhi(bb[1])};
        float a[4];
#pragma unroll
        for (int j = 0; j < 4; ++j) {
          const float h = r[nf][j] * rstd + (nmr * (c0[j] + c1[j]) + bias[j]);
          const float u = __builtin_amdgcn_exp2f(fminf(h * 1.4426950408889634f, 40.f));
          const float n = u * (u + 2.f);
          a[j] = h * n * __builtin_amdgcn_rcpf(n + 2.f);
        }
        u32x2 o = {cvtpk(a[0], a[1]), cvtpk(a[2], a[3])};
        *(u32x2*)(p.out0 + (size_t)trow * N + col) = o;
      }
    }
  } else {
#pragma unroll
    for (int nf = 0; nf < NF; ++nf) {
      const int col = n0 + (wn * NF + nf) * 16 + g * 4;
      const u32x2 rr = *(const u32x2*)(p.aux + (size_t)trow * N + col);
      u32x2 o = {cvtpk(r[nf][0] + bflo(rr[0]), r[nf][1] + bfhi(rr[0])), cvtpk(r[nf][2] + bflo(rr[1]), r[nf][3] + bfhi(rr[1]))};
      *(u32x2*)(p.out0 + (size_t)trow * N + col) = o;
    }
  }
}

constexpr int AT_K = 0, AT_V = 65536, AT_ONE = 131072, AT_Q = AT_ONE + 4096, AT_KM = AT_Q + 8192, AT_BYTES = AT_KM + 8 * 512;

template <bool FIRST>
DI void attn_pass(unsigned lds0, int lane, const bf16x8 qf, float negm, f32x16& o) {
  const int h = lane >> 5;
  unsigned kaddr = lds0 + AT_K + h * 512 + (lane & 31) * 16;
  const bool cl = (lane >> 4) & 1;
  const int qq = (lane & 15) >> 2, pp = lane & 3;
  unsigned vaddr = cl ? lds0 + AT_ONE + (4 * (h ^ 1) + qq) * 32 + pp * 8 : lds0 + AT_V + (4 * h + qq) * 32 + pp * 8;
  const unsigned vstep = cl ? 0u : 4096u;
  f32x16 cm;
#pragma unroll
  for (int i = 0; i < 16; ++i) cm[i] = negm;
#pragma unroll
  for (int i = 0; i < 16; ++i) o[i] = 0.f;
  for (int grp = 0; grp < 8; ++grp) {
    if (FIRST) {
      switch (grp) {
        case 0: break;
        case 1: wait_bar<12>(); break;
        case 2: wait_bar<10>(); break;
        case 3: wait_bar<8>(); break;
        case 4: wait_bar<6>(); break;
        case 5: wait_bar<4>(); break;
        case 6: wait_bar<2>(); break;
        default: wait_bar<0>(); break;
      }
    }
#pragma unroll
    for (int w = 0; w < 2; ++w) {
#pragma unroll
      for (int t = 0; t < 4; ++t) {
        const bf16x8 kf = ldsread16(kaddr + t * 1024);
        const s16x4 v0 = trread(vaddr + t * 1024), v1 = trread(vaddr + t * 1024 + 256);
        const s16x4 v2 = trread(vaddr + t * 1024 + 512), v3 = trread(vaddr + t * 1024 + 768);
        f32x16 s = __builtin_amdgcn_mfma_f32_32x32x16_bf16(kf, qf, cm, 0, 0, 0);
#pragma unroll
        for (int i = 0; i < 16; ++i) s[i] = __builtin_amdgcn_exp2f(s[i]);
        u32x4 p0 = {cvtpk(s[0], s[1]), cvtpk(s[2], s[3]), cvtpk(s[4], s[5]), cvtpk(s[6], s[7])};
        u32x4 p1 = {cvtpk(s[8], s[9]), cvtpk(s[10], s[11]), cvtpk(s[12], s[13]), cvtpk(s[14], s[15])};
        o = __builtin_amdgcn_mfma_f32_32x32x16_bf16(__builtin_shufflevector(v0, v1, 0, 1, 2, 3, 4, 5, 6, 7), __builtin_bit_cast(bf16x8, p0), o, 0, 0, 0);
        o = __builtin_amdgcn_mfma_f32_32x32x16_bf16(__builtin_shufflevector(v2, v3, 0, 1, 2, 3, 4, 5, 6, 7), __builtin_bit_cast(bf16x8, p1), o, 0, 0, 0);
      }
      kaddr += 4096; vaddr += vstep;
    }
  }
}

__global__ __launch_bounds__(512, 2) void attn_k(const u16* __restrict__ Q, const u16* __restrict__ K, const u16* __restrict__ V,
                                                  const float* __restrict__ kmax, u16* __restrict__ O) {
  __shared__ __attribute__((aligned(256))) char smem[AT_BYTES];
  const unsigned lds0 = (unsigned)(size_t)(LDSP(char))smem;
  const int tid = threadIdx.x, lane = tid & 63;
  const int wid = __builtin_amdgcn_readfirstlane(tid >> 6);
  const int bid = blockIdx.x;
  const int head = (bid & 7) * 4 + ((bid >> 3) & 3), qb = bid >> 5;
  const int q0 = qb * 256 + wid * 32;
  const unsigned l16 = lane * 16;
  glds16((const char*)Q + ((size_t)head * SEQ + q0) * 32, l16, lds0 + AT_Q + wid * 1024);
  if (lane < 32) glds16((const char*)kmax + head * 512, l16, lds0 + AT_KM + wid * 512);
  const char* kb = (const char*)K + (size_t)head * 65536 + wid * 1024;
  const char* vb = (const char*)V + (size_t)head * 65536 + wid * 1024;
#pragma unroll
  for (int i = 0; i < 8; ++i) {
    glds16(kb + i * 8192, l16, lds0 + AT_K + i * 8192 + wid * 1024);
    glds16(vb + i * 8192, l16, lds0 + AT_V + i * 8192 + wid * 1024);
  }
  *(u32x2*)(smem + AT_ONE + tid * 8) = u32x2{0x3f803f80u, 0x3f803f80u};
  wait_bar<14>();
  const bf16x8 qf = ldsread16(lds0 + AT_Q + wid * 1024 + (lane & 31) * 32 + (lane >> 5) * 16);
  float km = fmaxf(*(const float*)(smem + AT_KM + wid * 512 + lane * 4), *(const float*)(smem + AT_KM + wid * 512 + 256 + lane * 4));
#pragma unroll
  for (int d = 1; d < 64; d <<= 1) km = fmaxf(km, __shfl_xor(km, d, 64));
  float q2 = 0.f;
  {
    const u32x4 u = __builtin_bit_cast(u32x4, qf);
#pragma unroll
    for (int j = 0; j < 4; ++j) q2 = dot2(u[j], u[j], q2);
    q2 += xor32(q2);
  }
  float negm = -sqrtf(q2 * km) * 1.02f;
  f32x16 o;
  attn_pass<true>(lds0, lane, qf, negm, o);
  if (__any(!(o[8] >= 8.6736174e-19f && o[8] <= 3.0e38f))) {
    float m = -3.0e38f;
    unsigned kaddr = lds0 + AT_K + (lane >> 5) * 512 + (lane & 31) * 16;
    f32x16 z;
#pragma unroll
    for (int i = 0; i < 16; ++i) z[i] = 0.f;
    for (int t = 0; t < 64; ++t) {
      const f32x16 s = __builtin_amdgcn_mfma_f32_32x32x16_bf16(ldsread16(kaddr + t * 1024), qf, z, 0, 0, 0);
#pragma unroll
      for (int i = 0; i < 16; ++i) m = fmaxf(m, s[i]);
    }
    m = fmaxf(m, xor32(m));
    attn_pass<false>(lds0, lane, qf, -m, o);
  }
  const float rl = 1.0f / o[8];
  unsigned x0 = cvtpk(o[0] * rl, o[1] * rl), x1 = cvtpk(o[2] * rl, o[3] * rl);
  unsigned y0 = cvtpk(o[4] * rl, o[5] * rl), y1 = cvtpk(o[6] * rl, o[7] * rl);
  auto r0 = __builtin_amdgcn_permlane32_swap(x0, y0, false, false);
  auto r1 = __builtin_amdgcn_permlane32_swap(x1, y1, false, false);
  u32x4 ov = {r0[0], r1[0], r0[1], r1[1]};
  *(u32x4*)(O + (size_t)(q0 + (lane & 31)) * DM + head * 16 + (lane >> 5) * 8) = ov;
}

extern "C" void kernel_launch(void* const* d_in, const int* in_sizes, int n_in, void* d_out, int out_size, void* d_ws,
                              size_t ws_size, hipStream_t stream) {
  const u16* x = (const u16*)d_in[0];
  const u16* wqkv = (const u16*)d_in[1];
  const u16* wo = (const u16*)d_in[2];
  const u16* w1 = (const u16*)d_in[4];
  const u16* b1 = (const u16*)d_in[5];
  const u16* w2 = (const u16*)d_in[6];
  char* ws = (char*)d_ws;
  const size_t MB = 1 << 20;
  u16* Qb = (u16*)(ws + 0 * MB);
  u16* Kb = (u16*)(ws + 2 * MB);
  u16* Vb = (u16*)(ws + 4 * MB);
  u16* Ob = (u16*)(ws + 6 * MB);
  u16* X1 = (u16*)(ws + 8 * MB);
  u16* X2 = (u16*)(ws + 10 * MB);
  u16* Hb = (u16*)(ws + 12 * MB);
  float* kmax = (float*)(ws + 20 * MB);
  const u16* xin = x;
  for (int layer = 0; layer < 2; ++layer) {
    u16* xout = (layer == 0) ? X2 : (u16*)d_out;
    GemmArgs a;
    a = GemmArgs{xin, wqkv, nullptr, Qb, Kb, Vb, kmax};
    gemm_k<128, 96, 512, 1536, 4, 1, 5, EPI_QKV, 1><<<256, 512, 0, stream>>>(a);
    attn_k<<<256, 512, 0, stream>>>(Qb, Kb, Vb, kmax, Ob);
    a = GemmArgs{Ob, wo, xin, X1, nullptr, nullptr, nullptr};
    gemm_k<64, 64, 512, 512, 2, 2, 8, EPI_RES, 1><<<256, 512, 0, stream>>>(a);
    a = GemmArgs{X1, w1, b1, Hb, nullptr, nullptr, nullptr};
    gemm_k<128, 112, 512, 1792, 4, 1, 5, EPI_FFN1, 1><<<256, 512, 0, stream>>>(a);
    a = GemmArgs{Hb, w2, X1, xout, nullptr, nullptr, nullptr};
    gemm_k<64, 64, 1792, 512, 2, 2, 8, EPI_RES, 1><<<256, 512, 0, stream>>>(a);
    xin = X2;
  }
}
```

```cpp
#include <hip/hip_runtime.h>
#include <hip/hip_bf16.h>
#include <stdint.h>

typedef short bf16x8 __attribute__((ext_vector_type(8)));
typedef short s16x4 __attribute__((ext_vector_type(4)));
typedef __bf16 bf16x2 __attribute__((ext_vector_type(2)));
typedef float f32x4 __attribute__((ext_vector_type(4)));
typedef float f32x16 __attribute__((ext_vector_type(16)));
typedef unsigned u32x2 __attribute__((ext_vector_type(2)));
typedef unsigned u32x4 __attribute__((ext_vector_type(4)));
typedef unsigned short u16;
#define LDSP(T) __attribute__((address_space(3))) T*
#define DI __device__ __forceinline__

#define ABL 0
constexpr int SEQ = 2048, DM = 512, NH = 32, DH = 16, FF = 1792;

typedef float f32x2 __attribute__((ext_vector_type(2)));
DI unsigned cvtpk(float lo, float hi) { return __builtin_bit_cast(unsigned, __builtin_convertvector(f32x2{lo, hi}, bf16x2)); }
DI float bflo(unsigned u) { return __uint_as_float(u << 16); }
DI float bfhi(unsigned u) { return __uint_as_float(u & 0xffff0000u); }
DI float dot2(unsigned a, unsigned b, float c) {
  return __builtin_amdgcn_fdot2_f32_bf16(__builtin_bit_cast(bf16x2, a), __builtin_bit_cast(bf16x2, b), c, false);
}
DI void glds16(const void* sbase, unsigned voff, unsigned lds_addr) {
  unsigned sv;
  asm volatile("s_mov_b32 %0, m0\n\ts_mov_b32 m0, %3\n\ts_nop 0\n\tglobal_load_lds_dwordx4 %1, %2\n\ts_mov_b32 m0, %0"
               : "=&s"(sv) : "v"(voff), "s"(sbase), "s"(lds_addr) : "memory");
}
template <int NL> DI void glds16m(const void* sbase, unsigned voff, unsigned lds_addr) {
  unsigned sv;
  asm volatile("s_mov_b32 %0, m0\n\ts_mov_b32 m0, %3\n\ts_mov_b32 exec_hi, %4\n\tglobal_load_lds_dwordx4 %1, %2\n\ts_mov_b32 exec_hi, -1\n\ts_mov_b32 m0, %0"
               : "=&s"(sv) : "v"(voff), "s"(sbase), "s"(lds_addr), "i"((unsigned)((1ull << (NL - 32)) - 1)) : "memory");
}
#define SBAR() __builtin_amdgcn_sched_barrier(0)
#define PIN(x) asm volatile("" : "+v"(x))
#define MFMA32(a, b, c) __builtin_amdgcn_mfma_f32_32x32x16_bf16(a, b, c, 0, 0, 0)
#define CAT8(a, b) __builtin_shufflevector(a, b, 0, 1, 2, 3, 4, 5, 6, 7)
template <int N> DI void wait_bar() { asm volatile("s_waitcnt vmcnt(%0) lgkmcnt(0)\n\ts_barrier" ::"i"(N) : "memory"); }
DI void wait_bar_n(int n) { asm volatile("s_waitcnt vmcnt(%0) lgkmcnt(0)\n\ts_barrier" ::"i"(n) : "memory"); }
template <int N> DI void wait_vm() { asm volatile("s_waitcnt vmcnt(%0)" ::"i"(N) : "memory"); }
DI void lds_bar() { asm volatile("s_waitcnt lgkmcnt(0)\n\ts_barrier" ::: "memory"); }
#define STF 1
DI void st16(void* ptr, u32x4 v) {
  if (ABL & 32) { PIN(v); return; }
  if (STF == 0) *(u32x4*)ptr = v;
  else asm volatile("global_store_dwordx4 %0, %1, off sc1\n\ts_nop 1" ::"v"(ptr), "v"(v) : "memory");
}
DI u32x4 pair16(u32x2 a, u32x2 b) {
  auto r0 = __builtin_amdgcn_permlane16_swap(a[0], b[0], false, false);
  auto r1 = __builtin_amdgcn_permlane16_swap(a[1], b[1], false, false);
  return u32x4{r0[0], r1[0], r0[1], r1[1]};
}
DI void st16pair(u16* rowp, int g, u32x2 a, u32x2 b) { st16(rowp + (g & 1) * 16 + (g >> 1) * 8, pair16(a, b)); }
DI s16x4 trread(unsigned addr) {
  return __builtin_bit_cast(s16x4, __builtin_amdgcn_ds_read_tr16_b64_v4i16((LDSP(s16x4))(size_t)addr));
}
DI bf16x8 ldsread16(unsigned addr) { return *(const LDSP(bf16x8))(size_t)addr; }
DI float sum16(float v) { auto r = __builtin_amdgcn_permlane16_swap(__float_as_uint(v), __float_as_uint(v), false, false); return __uint_as_float(r[0]) + __uint_as_float(r[1]); }
DI float sum32(float v) { auto r = __builtin_amdgcn_permlane32_swap(__float_as_uint(v), __float_as_uint(v), false, false); return __uint_as_float(r[0]) + __uint_as_float(r[1]); }
DI float max16(float v) { auto r = __builtin_amdgcn_permlane16_swap(__float_as_uint(v), __float_as_uint(v), false, false); return fmaxf(__uint_as_float(r[0]), __uint_as_float(r[1])); }
DI float max32(float v) { auto r = __builtin_amdgcn_permlane32_swap(__float_as_uint(v), __float_as_uint(v), false, false); return fmaxf(__uint_as_float(r[0]), __uint_as_float(r[1])); }
template <int CTRL> DI float dppf(float v) { return __int_as_float(__builtin_amdgcn_update_dpp(0, __float_as_int(v), CTRL, 0xf, 0xf, true)); }
DI float rowmax16(float v) {
  v = fmaxf(v, dppf<0xB1>(v)); v = fmaxf(v, dppf<0x4E>(v)); v = fmaxf(v, dppf<0x141>(v)); v = fmaxf(v, dppf<0x140>(v)); return v;
}

enum { EPI_QKV = 0, EPI_FFN1 = 1, EPI_RES = 2 };

struct GemmArgs {
  const u16* A;
  const u16* B;
  const u16* aux;
  u16* out0;
  u16* out1;
  u16* out2;
  float* kmax;
  float* stats;
};

template <int BN> struct BL {
  static constexpr bool PADDED = (BN == 96);
  static constexpr int RB = BN * 2;
  static constexpr int GS = PADDED ? 784 : 4 * RB;
  static constexpr int STAGE = 16 * GS;
  static constexpr int NI = PADDED ? 16 : (64 * RB / 1024);
  static DI int sw(int row) { return BN == 64 ? (((row >> 1) & 1) | (((row >> 3) & 1) << 1)) : ((row & 3) | (((row >> 3) & 1) << 2)); }
};

template <int BM, int BN, int KTOT, int N, int WM, int WN, int NS, int SUB, int PD, int EPI, int MAP, bool STI>
__global__ __launch_bounds__(768, 3) void gemm_k(GemmArgs p) {
  constexpr int NK = KTOT / (64 * SUB);
  constexpr int ASTAGE = BM * 128;
  constexpr int BSTAGE = BL<BN>::STAGE;
  constexpr int SSTAGE = ASTAGE + BSTAGE;
  constexpr int STAGE = SSTAGE * SUB;
  constexpr int MF = BM / WM / 16, NF = BN / WN / 16;
  static_assert(WM * WN == 8, "8 compute waves");
  constexpr int LA = BM / 32;
  constexpr int LB = BL<BN>::NI / 4;
  constexpr int L = (LA + LB) * SUB;
  constexpr int D = NS - 1;
  constexpr bool LN = (EPI != EPI_RES);
  constexpr int XOFF = NS * STAGE;
  constexpr int EOFF = XOFF + 2048;
  constexpr int NE = (EPI == EPI_RES) ? 2 : (STI ? 0 : (EPI == EPI_FFN1 ? 5 : 4));
  constexpr int SMEM = EOFF + ((EPI == EPI_RES) ? 8192 : (STI ? 0 : 16384 + 1024));
  __shared__ __attribute__((aligned(256))) char smem[SMEM];
  const unsigned lds0 = (unsigned)(size_t)(LDSP(char))smem;
  float* csb = (float*)(smem + XOFF);
  float* st = csb + 256;

  const int tid = threadIdx.x, lane = tid & 63;
  const int wid = __builtin_amdgcn_readfirstlane(tid >> 6);
  const int l15 = lane & 15, g = lane >> 4;

  constexpr int GM = SEQ / BM, GN = N / BN;
  int pm, pn;
  {
    const int bid = blockIdx.x, xcd = bid & 7, idx = bid >> 3;
    if (MAP == 0) {
      constexpr int PER = GM / 8;
      pm = xcd * PER + idx / GN; pn = idx % GN;
    } else {
      constexpr int PM = GM / 4, PN = GN / 2;
      pm = (xcd >> 1) * PM + idx / PN; pn = (xcd & 1) * PN + idx % PN;
    }
  }
  const int m0 = pm * BM, n0 = pn * BN;
  const int bq = l15 >> 2, bp = lane & 3;
  auto bfrag_off = [&](int f) -> unsigned {
    if (BL<BN>::PADDED) return (2 * g) * BL<BN>::GS + bq * BL<BN>::RB + f * 32 + bp * 8;
    const int row = 8 * g + bq;
    return row * BL<BN>::RB + ((f ^ BL<BN>::sw(row)) * 32) + bp * 8;
  };

  if (wid >= 8) {
    const int w4 = wid - 8;
    __builtin_amdgcn_s_setprio(3);
    unsigned aoff[LA], boff[LB];
#pragma unroll
    for (int i = 0; i < LA; ++i) {
      const int ins = w4 * LA + i;
      const int row = ins * 8 + (lane >> 3), ch = (lane & 7) ^ ((row >> 1) & 7);
      aoff[i] = (unsigned)(row * KTOT * 2 + ch * 16);
    }
#pragma unroll
    for (int i = 0; i < LB; ++i) {
      const int ins = w4 * LB + i;
      constexpr int CPR = BN / 8;
      if (BL<BN>::PADDED) {
        const int row = ins * 4 + lane / CPR, ch = lane % CPR;
        boff[i] = (unsigned)(row * N * 2 + ch * 16);
      } else {
        const int row = ins * (64 / CPR) + lane / CPR;
        const int ch = (lane % CPR) ^ (BL<BN>::sw(row) << 1);
        boff[i] = (unsigned)(row * N * 2 + ch * 16);
      }
    }
    const char* abase = (const char*)p.A + (size_t)m0 * KTOT * 2;
    const char* bbase = (const char*)p.B + (size_t)n0 * 2;
    auto issue = [&](int s) {
      if (ABL & 1) return;
#pragma unroll
      for (int u = 0; u < SUB; ++u) {
        const int ss = s * SUB + u;
        const unsigned slot = lds0 + (s % NS) * STAGE + u * SSTAGE;
#pragma unroll
        for (int i = 0; i < LA; ++i) glds16(abase + ss * 128, aoff[i], slot + (w4 * LA + i) * 1024);
#pragma unroll
        for (int i = 0; i < LB; ++i) {
          if (BL<BN>::PADDED) glds16m<BL<BN>::PADDED ? 4 * (BN / 8) : 64>(bbase + (size_t)ss * 64 * N * 2, boff[i], slot + ASTAGE + (w4 * LB + i) * BL<BN>::GS);
          else glds16(bbase + (size_t)ss * 64 * N * 2, boff[i], slot + ASTAGE + (w4 * LB + i) * 1024);
        }
      }
    };
    auto extras = [&]() {
      if (ABL & 1) return;
      if (EPI == EPI_RES) {
#pragma unroll
        for (int i = 0; i < 2; ++i)
        {
          const int row = (w4 * 2 + i) * 8 + (lane >> 3), ch = (lane & 7) ^ ((row >> 1) & 7);
          glds16((const char*)p.aux + ((size_t)m0 * N + n0) * 2, (unsigned)(row * N * 2 + ch * 16), lds0 + EOFF + (w4 * 2 + i) * 1024);
        }
      } else if (!STI) {
#pragma unroll
        for (int i = 0; i < 4; ++i) glds16((const char*)p.stats + (size_t)m0 * 128 + (w4 * 4 + i) * 1024, lane * 16, lds0 + EOFF + (w4 * 4 + i) * 1024);
        if (EPI == EPI_FFN1) glds16m<32>((const char*)p.aux + n0 * 2, (lane & 15) * 16, lds0 + EOFF + 16384);
      }
    };
    auto upto = [](int j) { const int a = 2 + 2 * j, b = j + D; const int c = a < b ? a : b; return c < NK ? c : NK; };
#pragma unroll
    for (int s = 0; s < upto(0); ++s) issue(s);
    if (upto(0) == NK) extras();
    const bf16x8 ONES = __builtin_bit_cast(bf16x8, u32x4{0x3f803f80u, 0x3f803f80u, 0x3f803f80u, 0x3f803f80u});
    const unsigned c_rd0 = lds0 + ASTAGE + bfrag_off(w4), c_rd1 = lds0 + ASTAGE + bfrag_off(w4 + 4);
    const unsigned a_rd = lds0 + (w4 * 32 + l15) * 128 + ((g ^ (l15 >> 1)) * 16);
    f32x4 cs0 = {0.f, 0.f, 0.f, 0.f}, cs1 = cs0, rs[2] = {cs0, cs0}, rq[2] = {cs0, cs0};
#pragma unroll
    for (int j = 0; j < NK; ++j) {
      const int behind = (upto(j) - 1 - j) * L + (upto(j) == NK ? NE : 0);
      wait_bar_n(behind);
#pragma unroll
      for (int s = upto(j); s < upto(j + 1); ++s) issue(s);
      if (upto(j) < NK && upto(j + 1) == NK) extras();
      if (LN && !(ABL & 16)) {
        const unsigned so = (j % NS) * STAGE;
#pragma unroll
        for (int ks = 0; ks < 2; ++ks) {
          const unsigned o = so + ks * 8 * BL<BN>::GS;
          cs0 = __builtin_amdgcn_mfma_f32_16x16x32_bf16(CAT8(trread(c_rd0 + o), trread(c_rd0 + o + BL<BN>::GS)), ONES, cs0, 0, 0, 0);
          cs1 = __builtin_amdgcn_mfma_f32_16x16x32_bf16(CAT8(trread(c_rd1 + o), trread(c_rd1 + o + BL<BN>::GS)), ONES, cs1, 0, 0, 0);
          if (STI) {
#pragma unroll
            for (int mf = 0; mf < 2; ++mf) {
              const bf16x8 a = ldsread16((a_rd ^ (ks * 64)) + so + mf * 2048);
              rs[mf] = __builtin_amdgcn_mfma_f32_16x16x32_bf16(ONES, a, rs[mf], 0, 0, 0);
              rq[mf] = __builtin_amdgcn_mfma_f32_16x16x32_bf16(a, a, rq[mf], 0, 0, 0);
            }
          }
        }
        PIN(cs0); PIN(cs1);
        if (STI) { PIN(rs[0]); PIN(rs[1]); PIN(rq[0]); PIN(rq[1]); }
      }
    }
    if (LN) {
      if (l15 == 0) {
        *(f32x4*)(csb + w4 * 16 + g * 4) = cs0;
        *(f32x4*)(csb + 64 + w4 * 16 + g * 4) = cs1;
      }
      if (STI) {
#pragma unroll
        for (int mf = 0; mf < 2; ++mf) {
          const int jj = l15 & 3;
          const float d = (jj == 0) ? rq[mf][0] : (jj == 1) ? rq[mf][1] : (jj == 2) ? rq[mf][2] : rq[mf][3];
          if (g == (l15 >> 2)) *(f32x2*)(st + (w4 * 32 + mf * 16 + l15) * 2) = f32x2{rs[mf][0], d};
        }
      } else {
        wait_vm<0>();
#pragma unroll
        for (int i = 0; i < 4; ++i) {
          const f32x4 v = *(const f32x4*)(smem + EOFF + (w4 * 4 + i) * 1024 + lane * 16);
          float a = v[0] + v[2], b = v[1] + v[3];
          a += dppf<0xB1>(a); b += dppf<0xB1>(b); a += dppf<0x4E>(a); b += dppf<0x4E>(b); a += dppf<0x141>(a); b += dppf<0x141>(b);
          if ((lane & 7) == 0) *(f32x2*)(st + (w4 * 32 + i * 8 + (lane >> 3)) * 2) = f32x2{a, b};
        }
      }
    }
    wait_bar<0>();
    return;
  }

  const int wm = wid % WM, wn = wid / WM;
  const int trow = m0 + wm * (MF * 16) + l15;
  const int ncol = n0 + wn * NF * 16;
  u32x2 pre[MF][NF];
  float rstd[MF], nmr[MF];
  auto mkstats = [&](int mf, float s1, float s2) {
    const float mean = s1 * (1.f / KTOT);
    const float var = fmaxf(s2 * (1.f / KTOT) - mean * mean, 0.f);
    rstd[mf] = __builtin_amdgcn_rsqf(var + 1e-5f);
    nmr[mf] = -mean * rstd[mf];
  };
  const unsigned a_rd = lds0 + (wm * (MF * 16) + l15) * 128 + ((g ^ (l15 >> 1)) * 16);
  unsigned b_rd[BL<BN>::PADDED ? 1 : NF];
#pragma unroll
  for (int nf = 0; nf < (BL<BN>::PADDED ? 1 : NF); ++nf) b_rd[nf] = lds0 + ASTAGE + bfrag_off(wn * NF + nf);
  f32x4 acc[MF][NF];
#pragma unroll
  for (int a = 0; a < MF; ++a)
#pragma unroll
    for (int b = 0; b < NF; ++b) acc[a][b] = f32x4{0.f, 0.f, 0.f, 0.f};

  constexpr int KS = 2 * SUB, GPS = KS / PD, NG = NK * GPS;
  bf16x8 af[2][PD][MF], bfr[2][PD][NF];
  auto rdfrags = [&](int gi) {
    if ((ABL & 2) && gi > 1) return;
    const int pb = gi & 1, s = gi / GPS;
#pragma unroll
    for (int q = 0; q < PD; ++q) {
      const int k = (gi % GPS) * PD + q;
      const unsigned so = (s % NS) * STAGE + (k >> 1) * SSTAGE;
#pragma unroll
      for (int mf = 0; mf < MF; ++mf) af[pb][q][mf] = ldsread16((a_rd ^ ((k & 1) * 64)) + so + mf * 2048);
#pragma unroll
      for (int nf = 0; nf < NF; ++nf) {
        const unsigned o = (BL<BN>::PADDED ? b_rd[0] + nf * 32 : b_rd[nf]) + so + (k & 1) * 8 * BL<BN>::GS;
        bfr[pb][q][nf] = CAT8(trread(o), trread(o + BL<BN>::GS));
      }
    }
  };
  lds_bar();
  rdfrags(0);
#pragma unroll
  for (int gi = 0; gi < NG; ++gi) {
    if (gi + 1 < NG) {
      if ((gi + 1) % GPS == 0) {
        lds_bar();
      }
      SBAR();
      rdfrags(gi + 1);
    }
    const int pb = gi & 1;
    if (!(ABL & 8) || gi < 2)
#pragma unroll
    for (int q = 0; q < PD; ++q)
#pragma unroll
      for (int nf = 0; nf < NF; ++nf)
#pragma unroll
        for (int mf = 0; mf < MF; ++mf)
          acc[mf][nf] = __builtin_amdgcn_mfma_f32_16x16x32_bf16(bfr[pb][q][nf], af[pb][q][mf], acc[mf][nf], 0, 0, 0);
    if (gi + 1 < NG) {
      constexpr int NM = PD * MF * NF, ND = PD * (MF + 2 * NF);
      constexpr int PER = (ND + NM - 1) / NM;
#pragma unroll
      for (int i = 0; i < NM; ++i) {
        __builtin_amdgcn_sched_group_barrier(0x100, PER, 0);
        __builtin_amdgcn_sched_group_barrier(0x8, 1, 0);
      }
    }
#pragma unroll
    for (int mf = 0; mf < MF; ++mf)
#pragma unroll
      for (int nf = 0; nf < NF; ++nf) PIN(acc[mf][nf]);
  }
  lds_bar();
  if (ABL & 4) { if (acc[0][0][0] == 123.456f) p.out0[0] = 1; return; }
  if (LN) {
    f32x4 c0[NF];
#pragma unroll
    for (int nf = 0; nf < NF; ++nf) c0[nf] = *(const f32x4*)(csb + (wn * NF + nf) * 16 + g * 4);
#pragma unroll
    for (int mf = 0; mf < MF; ++mf) { const f32x2 a = *(const f32x2*)(st + (wm * (MF * 16) + mf * 16 + l15) * 2); mkstats(mf, a[0], a[1]); }
    if (EPI == EPI_FFN1) {
#pragma unroll
      for (int nf = 0; nf < NF; ++nf) pre[0][nf] = *(const u32x2*)(smem + EOFF + 16384 + ((wn * NF + nf) * 16 + g * 4) * 2);
    }
    if (EPI == EPI_QKV) {
      const int tk = trow + (g & 1) * 16;
      const unsigned lin = tk * 16 + (g >> 1) * 8;
      const unsigned kof = (tk >> 5) * 512 + (g >> 1) * 256 + (tk & 31) * 8;
#pragma unroll
      for (int nf = 0; nf < NF; ++nf) {
        const int hh = (ncol >> 4) + nf;
        const bool isq = hh < 32, isk = (hh >> 5) == 1;
        const float sc = isq ? 0.25f * 1.4426950408889634f : 1.f;
        u16* base = p.out0 + (size_t)hh * (SEQ * 16);
        u32x2 o[MF];
#pragma unroll
        for (int mf = 0; mf < MF; ++mf) {
          acc[mf][nf] = acc[mf][nf] * (rstd[mf] * sc) + c0[nf] * (nmr[mf] * sc);
          const f32x4 y = acc[mf][nf];
          o[mf] = u32x2{cvtpk(y[0], y[1]), cvtpk(y[2], y[3])};
        }
        st16(base + (isk ? kof : lin), pair16(o[0], o[MF - 1]));
      }
    } else {
      u32x2 o[MF][NF];
#pragma unroll
      for (int nf = 0; nf < NF; ++nf) {
        const f32x4 bias = {bflo(pre[0][nf][0]), bfhi(pre[0][nf][0]), bflo(pre[0][nf][1]), bfhi(pre[0][nf][1])};
#pragma unroll
        for (int mf = 0; mf < MF; ++mf) {
          const f32x4 h = acc[mf][nf] * rstd[mf] + (c0[nf] * nmr[mf] + bias);
          const f32x4 t = h * 1.4426950408889634f;
          f32x4 u, rc;
#pragma unroll
          for (int j = 0; j < 4; ++j) u[j] = __builtin_amdgcn_exp2f(t[j]);
          const f32x4 d = u * (u * -0.5f - 1.f) - 1.f;
#pragma unroll
          for (int j = 0; j < 4; ++j) rc[j] = __builtin_amdgcn_rcpf(d[j]);
          const f32x4 a = h * rc + h;
          o[mf][nf] = u32x2{cvtpk(a[0], a[1]), cvtpk(a[2], a[3])};
          if (nf & 1) st16pair(p.out0 + (size_t)(trow + mf * 16) * N + ncol + (nf - 1) * 16, g, o[mf][nf - 1], o[mf][nf]);
        }
      }
    }
  } else {
#pragma unroll
    for (int mf = 0; mf < MF; ++mf) {
      const int tok = trow + mf * 16;
      float s1 = 0.f, s2 = 0.f;
      u32x2 o[NF];
#pragma unroll
      for (int nf = 0; nf < NF; ++nf) {
        const u32x2 rr = *(const u32x2*)(smem + EOFF + (wm * (MF * 16) + mf * 16 + l15) * 128 + ((((wn * NF + nf) * 2 + (g >> 1)) ^ (l15 >> 1)) * 16) + (g & 1) * 8);
        const f32x4 r = acc[mf][nf];
        o[nf] = u32x2{cvtpk(r[0] + bflo(rr[0]), r[1] + bfhi(rr[0])), cvtpk(r[2] + bflo(rr[1]), r[3] + bfhi(rr[1]))};
        const float e0 = bflo(o[nf][0]), e1 = bfhi(o[nf][0]), e2 = bflo(o[nf][1]), e3 = bfhi(o[nf][1]);
        s1 += (e0 + e1) + (e2 + e3);
        s2 += (e0 * e0 + e1 * e1) + (e2 * e2 + e3 * e3);
        if (nf & 1) st16pair(p.out0 + (size_t)tok * N + ncol + (nf - 1) * 16, g, o[nf - 1], o[nf]);
      }
      if (STI) {
        s1 = sum32(sum16(s1)); s2 = sum32(sum16(s2));
        if (g == 0) *(f32x2*)(p.stats + tok * 32 + (ncol >> 5) * 2) = f32x2{s1, s2};
      }
    }
  }
}

constexpr int AT_K = 0, AT_V = 65536, AT_ONE = 131072, AT_X = AT_ONE + 6144, AT_Q = AT_X + 256, AT_PF = AT_Q + 8 * 9 * 256, AT_BYTES = AT_PF + 1024;

#define ATT_HALF(SC, OO, VC, MID)                                                                            \
  {                                                                                                          \
    _Pragma("unroll") for (int i = 0; i < 4; ++i) SC[i] = __builtin_amdgcn_exp2f(SC[i]);                     \
    SBAR(); MID; SBAR();                                                                                     \
    _Pragma("unroll") for (int i = 4; i < 9; ++i) SC[i] = __builtin_amdgcn_exp2f(SC[i]);                     \
    u32x4 p0 = {cvtpk(SC[0], SC[1]), cvtpk(SC[2], SC[3]), cvtpk(SC[4], SC[5]), cvtpk(SC[6], SC[7])};         \
    PIN(p0); SBAR();                                                                                         \
    SC[9] = __builtin_amdgcn_exp2f(SC[9]); SBAR();                                                           \
    OO = MFMA32(CAT8(VC[0], VC[1]), __builtin_bit_cast(bf16x8, p0), OO); SBAR();                             \
    _Pragma("unroll") for (int i = 10; i < 16; ++i) SC[i] = __builtin_amdgcn_exp2f(SC[i]);                   \
    u32x4 p1 = {cvtpk(SC[8], SC[9]), cvtpk(SC[10], SC[11]), cvtpk(SC[12], SC[13]), cvtpk(SC[14], SC[15])};   \
    OO = MFMA32(CAT8(VC[2], VC[3]), __builtin_bit_cast(bf16x8, p1), OO); SBAR();                             \
  }
#define ATT_TILE(VC, VN, T)                                                                                  \
  ATT_HALF(s0, o0, VC, { s1 = MFMA32(kf, qf1, cm1); PIN(s1); kf = ldsread16(kaddr + ((T) + 1) * 1024);       \
       VN[0] = trread(vaddr + ((T) + 1) * 1024); VN[1] = trread(vaddr + ((T) + 1) * 1024 + 256); })          \
  ATT_HALF(s1, o1, VC, { s0 = MFMA32(kf, qf0, cm0); PIN(s0);                                                 \
       VN[2] = trread(vaddr + ((T) + 1) * 1024 + 512); VN[3] = trread(vaddr + ((T) + 1) * 1024 + 768); })

template <bool FAST>
DI void attn_pass(unsigned lds0, int lane, int ks, const bf16x8 qf0, const bf16x8 qf1, float nm0, float nm1, f32x16& o0, f32x16& o1,
                  const char* kb, const char* vb, unsigned kvdst, const char* pf, unsigned pfoff, unsigned pfstep) {
  const int h = lane >> 5;
  unsigned kaddr = lds0 + AT_K + ks * 32768 + h * 512 + (lane & 31) * 16;
  const bool cl = (lane >> 4) & 1;
  const int qq = (lane & 15) >> 2, pp = lane & 3;
  unsigned vaddr = cl ? lds0 + AT_ONE + (4 * (h ^ 1) + qq) * 32 + pp * 8 : lds0 + AT_V + ks * 32768 + (4 * h + qq) * 32 + pp * 8;
  const unsigned vstep = cl ? 0u : 4096u;
  f32x16 cm0, cm1, s0, s1;
  s16x4 vA[4], vB[4];
#pragma unroll
  for (int i = 0; i < 16; ++i) { cm0[i] = FAST ? 0.f : nm0; cm1[i] = FAST ? 0.f : nm1; o0[i] = 0.f; o1[i] = 0.f; }
  bf16x8 kf = ldsread16(kaddr);
  s0 = MFMA32(kf, qf0, cm0);
  vA[0] = trread(vaddr); vA[1] = trread(vaddr + 256); vA[2] = trread(vaddr + 512); vA[3] = trread(vaddr + 768);
  for (int it = 0; it < 8; ++it) {
    PIN(kaddr); PIN(vaddr);
    if (FAST && it < 6) {
      glds16(kb + (it + 2) * 4096, lane * 16, kvdst + AT_K + (it + 2) * 4096);
      glds16(vb + (it + 2) * 4096, lane * 16, kvdst + AT_V + (it + 2) * 4096);
    }
    if (FAST) { glds16(pf, pfoff, lds0 + AT_PF); pfoff += pfstep; }
    ATT_TILE(vA, vB, 0) ATT_TILE(vB, vA, 1) ATT_TILE(vA, vB, 2) ATT_TILE(vB, vA, 3)
    kaddr += 4096; vaddr += vstep;
    if (FAST) wait_bar<1>(); else wait_bar<0>();
  }
}

__global__ __launch_bounds__(512, 2) void attn_k(const u16* __restrict__ Q, const u16* __restrict__ K, const u16* __restrict__ V,
                                                  u16* __restrict__ O,
                                                  const u16* __restrict__ wo, const u16* __restrict__ w1, const u16* __restrict__ w2) {
  __shared__ __attribute__((aligned(256))) char smem[AT_BYTES];
  const unsigned lds0 = (unsigned)(size_t)(LDSP(char))smem;
  const int tid = threadIdx.x, lane = tid & 63;
  const int wid = __builtin_amdgcn_readfirstlane(tid >> 6);
  const int wq = wid & 3, ks = wid >> 2;
  const int bid = blockIdx.x;
  const int head = (bid & 7) * 4 + ((bid >> 3) & 3), qb = bid >> 5;
  const unsigned l16 = lane * 16;
  const char* kb = (const char*)K + (size_t)head * 65536 + (ks * 32 + wq) * 1024;
  const char* vb = (const char*)V + (size_t)head * 65536 + (ks * 32 + wq) * 1024;
  const unsigned kvdst = lds0 + (ks * 32 + wq) * 1024;
  glds16((const char*)Q + ((size_t)head * SEQ + qb * 256 + wid * 32) * 32, l16, lds0 + AT_Q + wid * 1024);
#pragma unroll
  for (int i = 0; i < 2; ++i) {
    glds16(kb + i * 4096, l16, kvdst + AT_K + i * 4096);
    glds16(vb + i * 4096, l16, kvdst + AT_V + i * 4096);
  }
  if (tid < 384) *(u32x4*)(smem + AT_ONE + tid * 16) = u32x4{(tid & 1) ? 0u : 0x3f80u, 0u, 0u, 0u};
  if (tid == 511) *(unsigned*)(smem + AT_X) = 0u;
  wait_bar<0>();
  const bf16x8 qf0 = ldsread16(lds0 + AT_Q + wq * 2048 + (lane & 31) * 32 + (lane >> 5) * 16);
  const bf16x8 qf1 = ldsread16(lds0 + AT_Q + wq * 2048 + 1024 + (lane & 31) * 32 + (lane >> 5) * 16);
  const char* pf; unsigned pfoff, pfstep;
  {
    const int u = (bid >> 3) * 8 + wid, half = bid & 1;
    if (u < 32) { pf = (const char*)wo + half * 512; pfoff = (u * 16 + (lane >> 5)) * 1024 + (lane & 31) * 16; pfstep = 2048; }
    else if (u < 144) { pf = (const char*)w2 + half * 512; pfoff = ((u - 32) * 16 + (lane >> 5)) * 1024 + (lane & 31) * 16; pfstep = 2048; }
    else {
      const unsigned c0 = (u - 144) * 64 + lane;
      pf = (const char*)w1 + half * 1792; pfoff = (c0 / 112) * 3584 + (c0 % 112) * 16; pfstep = 64 * 3584;
    }
  }
  f32x16 o0, o1;
  attn_pass<true>(lds0, lane, ks, qf0, qf1, 0.f, 0.f, o0, o1, kb, vb, kvdst, pf, pfoff, pfstep);
  wait_vm<0>();
  float* xb = (float*)(smem + AT_Q);
  if (lane < 32 && !(o0[8] >= 8.67e-19f && o0[8] <= 1.26e30f && o1[8] >= 8.67e-19f && o1[8] <= 1.26e30f)) *(unsigned*)(smem + AT_X) = 1u;
#pragma unroll
  for (int i = 0; i < 9; ++i) xb[(wid * 9 + i) * 64 + lane] = ks ? o0[i] : o1[i];
  lds_bar();
  if (__builtin_amdgcn_readfirstlane(*(const volatile unsigned*)(smem + AT_X)) != 0u) {
    float m0 = -3.0e38f, m1 = -3.0e38f;
    const unsigned kaddr = lds0 + AT_K + (lane >> 5) * 512 + (lane & 31) * 16;
    f32x16 z;
#pragma unroll
    for (int i = 0; i < 16; ++i) z[i] = 0.f;
    for (int t = 0; t < 64; ++t) {
      const bf16x8 kf = ldsread16(kaddr + t * 1024);
      const f32x16 sa = MFMA32(kf, qf0, z), sb = MFMA32(kf, qf1, z);
#pragma unroll
      for (int i = 0; i < 16; ++i) { m0 = fmaxf(m0, sa[i]); m1 = fmaxf(m1, sb[i]); }
    }
    m0 = max32(m0); m1 = max32(m1);
    attn_pass<false>(lds0, lane, ks, qf0, qf1, -m0, -m1, o0, o1, kb, vb, kvdst, pf, pfoff, pfstep);
#pragma unroll
    for (int i = 0; i < 9; ++i) xb[(wid * 9 + i) * 64 + lane] = ks ? o0[i] : o1[i];
    lds_bar();
  }
  f32x16 o = ks ? o1 : o0;
#pragma unroll
  for (int i = 0; i < 9; ++i) o[i] += xb[((wid ^ 4) * 9 + i) * 64 + lane];
  auto lr = __builtin_amdgcn_permlane32_swap(__float_as_uint(o[8]), __float_as_uint(o[8]), false, false);
  const float rl = __builtin_amdgcn_rcpf(__uint_as_float(lr[0]));
  unsigned x0 = cvtpk(o[0] * rl, o[1] * rl), x1 = cvtpk(o[2] * rl, o[3] * rl);
  unsigned y0 = cvtpk(o[4] * rl, o[5] * rl), y1 = cvtpk(o[6] * rl, o[7] * rl);
  auto r0 = __builtin_amdgcn_permlane32_swap(x0, y0, false, false);
  auto r1 = __builtin_amdgcn_permlane32_swap(x1, y1, false, false);
  u32x4 ov = {r0[0], r1[0], r0[1], r1[1]};
  const int q0 = qb * 256 + wq * 64 + ks * 32;
  *(u32x4*)(O + (size_t)(q0 + (lane & 31)) * DM + head * 16 + (lane >> 5) * 8) = ov;
}

extern "C" void kernel_launch(void* const* d_in, const int* in_sizes, int n_in, void* d_out, int out_size, void* d_ws,
                              size_t ws_size, hipStream_t stream) {
  const u16* x = (const u16*)d_in[0];
  const u16* wqkv = (const u16*)d_in[1];
  const u16* wo = (const u16*)d_in[2];
  const u16* w1 = (const u16*)d_in[4];
  const u16* b1 = (const u16*)d_in[5];
  const u16* w2 = (const u16*)d_in[6];
  char* ws = (char*)d_ws;
  const size_t MB = 1 << 20;
  u16* Qb = (u16*)(ws + 0 * MB);
  u16* Kb = (u16*)(ws + 2 * MB);
  u16* Vb = (u16*)(ws + 4 * MB);
  u16* Ob = (u16*)(ws + 6 * MB);
  u16* X1 = (u16*)(ws + 8 * MB);
  u16* X2 = (u16*)(ws + 10 * MB);
  u16* Hb = (u16*)(ws + 12 * MB);
  float* kmax = (float*)(ws + 20 * MB);
  float* st1 = (float*)(ws + 21 * MB);
  float* st2 = (float*)(ws + 22 * MB);
  const u16* xin = x;
  for (int layer = 0; layer < 2; ++layer) {
    u16* xout = (layer == 0) ? X2 : (u16*)d_out;
    GemmArgs a;
    a = GemmArgs{xin, wqkv, nullptr, Qb, Kb, Vb, kmax, st2};
    if (layer == 0) gemm_k<128, 96, 512, 1536, 4, 2, 4, 1, 1, EPI_QKV, 1, true><<<256, 768, 0, stream>>>(a);
    else gemm_k<128, 96, 512, 1536, 4, 2, 4, 1, 1, EPI_QKV, 1, false><<<256, 768, 0, stream>>>(a);
    attn_k<<<256, 512, 0, stream>>>(Qb, Kb, Vb, Ob, wo, w1, w2);
    a = GemmArgs{Ob, wo, xin, X1, nullptr, nullptr, nullptr, st1};
    gemm_k<64, 64, 512, 512, 4, 2, 4, 2, 4, EPI_RES, 1, true><<<256, 768, 0, stream>>>(a);
    a = GemmArgs{X1, w1, b1, Hb, nullptr, nullptr, nullptr, st1};
    gemm_k<128, 128, 512, 1792, 4, 2, 4, 1, 1, EPI_FFN1, 1, false><<<224, 768, 0, stream>>>(a);
    a = GemmArgs{Hb, w2, X1, xout, nullptr, nullptr, nullptr, st2};
    gemm_k<64, 64, 1792, 512, 4, 2, 4, 2, 4, EPI_RES, 1, true><<<256, 768, 0, stream>>>(a);
    xin = X2;
  }
}
```

```cpp
#include <hip/hip_runtime.h>
#include <hip/hip_bf16.h>
#include <stdint.h>

typedef short bf16x8 __attribute__((ext_vector_type(8)));
typedef short s16x4 __attribute__((ext_vector_type(4)));
typedef __bf16 bf16x2 __attribute__((ext_vector_type(2)));
typedef float f32x4 __attribute__((ext_vector_type(4)));
typedef float f32x16 __attribute__((ext_vector_type(16)));
typedef unsigned u32x2 __attribute__((ext_vector_type(2)));
typedef unsigned u32x4 __attribute__((ext_vector_type(4)));
typedef unsigned short u16;
#define LDSP(T) __attribute__((address_space(3))) T*
#define DI __device__ __forceinline__

#define NOCP 0
#define ABL 0
constexpr int SEQ = 2048, DM = 512, NH = 32, DH = 16, FF = 1792;

typedef float f32x2 __attribute__((ext_vector_type(2)));
DI unsigned cvtpk(float lo, float hi) { return __builtin_bit_cast(unsigned, __builtin_convertvector(f32x2{lo, hi}, bf16x2)); }
DI float bflo(unsigned u) { return __uint_as_float(u << 16); }
DI float bfhi(unsigned u) { return __uint_as_float(u & 0xffff0000u); }
DI float dot2(unsigned a, unsigned b, float c) {
  return __builtin_amdgcn_fdot2_f32_bf16(__builtin_bit_cast(bf16x2, a), __builtin_bit_cast(bf16x2, b), c, false);
}
DI void glds16(const void* sbase, unsigned voff, unsigned lds_addr) {
  unsigned sv;
  asm volatile("s_mov_b32 %0, m0\n\ts_mov_b32 m0, %3\n\ts_nop 0\n\tglobal_load_lds_dwordx4 %1, %2\n\ts_mov_b32 m0, %0"
               : "=&s"(sv) : "v"(voff), "s"(sbase), "s"(lds_addr) : "memory");
}
template <int NL> DI void glds16m(const void* sbase, unsigned voff, unsigned lds_addr) {
  unsigned sv;
  asm volatile("s_mov_b32 %0, m0\n\ts_mov_b32 m0, %3\n\ts_mov_b32 exec_hi, %4\n\tglobal_load_lds_dwordx4 %1, %2\n\ts_mov_b32 exec_hi, -1\n\ts_mov_b32 m0, %0"
               : "=&s"(sv) : "v"(voff), "s"(sbase), "s"(lds_addr), "i"((unsigned)((1ull << (NL - 32)) - 1)) : "memory");
}
#define SBAR() __builtin_amdgcn_sched_barrier(0)
#define PIN(x) asm volatile("" : "+v"(x))
#define MFMA32(a, b, c) __builtin_amdgcn_mfma_f32_32x32x16_bf16(a, b, c, 0, 0, 0)
#define CAT8(a, b) __builtin_shufflevector(a, b, 0, 1, 2, 3, 4, 5, 6, 7)
template <int N> DI void wait_bar() { asm volatile("s_waitcnt vmcnt(%0) lgkmcnt(0)\n\ts_barrier" ::"i"(N) : "memory"); }
DI void wait_bar_n(int n) { asm volatile("s_waitcnt vmcnt(%0) lgkmcnt(0)\n\ts_barrier" ::"i"(n) : "memory"); }
template <int N> DI void wait_vm() { asm volatile("s_waitcnt vmcnt(%0)" ::"i"(N) : "memory"); }
DI void lds_bar() { asm volatile("s_waitcnt lgkmcnt(0)\n\ts_barrier" ::: "memory"); }
#define STF 1
DI void st16(void* ptr, u32x4 v) {
  if (ABL & 32) { PIN(v); return; }
  if (STF == 0) *(u32x4*)ptr = v;
  else asm volatile("global_store_dwordx4 %0, %1, off sc1\n\ts_nop 1" ::"v"(ptr), "v"(v) : "memory");
}
DI u32x4 pair16(u32x2 a, u32x2 b) {
  auto r0 = __builtin_amdgcn_permlane16_swap(a[0], b[0], false, false);
  auto r1 = __builtin_amdgcn_permlane16_swap(a[1], b[1], false, false);
  return u32x4{r0[0], r1[0], r0[1], r1[1]};
}
DI void st16pair(u16* rowp, int g, u32x2 a, u32x2 b) { st16(rowp + (g & 1) * 16 + (g >> 1) * 8, pair16(a, b)); }
DI s16x4 trread(unsigned addr) {
  return __builtin_bit_cast(s16x4, __builtin_amdgcn_ds_read_tr16_b64_v4i16((LDSP(s16x4))(size_t)addr));
}
DI bf16x8 ldsread16(unsigned addr) { return *(const LDSP(bf16x8))(size_t)addr; }
DI float sum16(float v) { auto r = __builtin_amdgcn_permlane16_swap(__float_as_uint(v), __float_as_uint(v), false, false); return __uint_as_float(r[0]) + __uint_as_float(r[1]); }
DI float sum32(float v) { auto r = __builtin_amdgcn_permlane32_swap(__float_as_uint(v), __float_as_uint(v), false, false); return __uint_as_float(r[0]) + __uint_as_float(r[1]); }
DI float max16(float v) { auto r = __builtin_amdgcn_permlane16_swap(__float_as_uint(v), __float_as_uint(v), false, false); return fmaxf(__uint_as_float(r[0]), __uint_as_float(r[1])); }
DI float max32(float v) { auto r = __builtin_amdgcn_permlane32_swap(__float_as_uint(v), __float_as_uint(v), false, false); return fmaxf(__uint_as_float(r[0]), __uint_as_float(r[1])); }
template <int CTRL> DI float dppf(float v) { return __int_as_float(__builtin_amdgcn_update_dpp(0, __float_as_int(v), CTRL, 0xf, 0xf, true)); }
DI float rowmax16(float v) {
  v = fmaxf(v, dppf<0xB1>(v)); v = fmaxf(v, dppf<0x4E>(v)); v = fmaxf(v, dppf<0x141>(v)); v = fmaxf(v, dppf<0x140>(v)); return v;
}

enum { EPI_QKV = 0, EPI_FFN1 = 1, EPI_RES = 2 };

struct GemmArgs {
  const u16* A;
  const u16* B;
  const u16* aux;
  u16* out0;
  u16* out1;
  u16* out2;
  float* kmax;
  float* stats;
};

template <int BN> struct BL {
  static constexpr bool PADDED = (BN == 96);
  static constexpr int RB = BN * 2;
  static constexpr int GS = PADDED ? 784 : 4 * RB;
  static constexpr int STAGE = 16 * GS;
  static constexpr int NI = PADDED ? 16 : (64 * RB / 1024);
  static DI int sw(int row) { return BN == 64 ? (((row >> 1) & 1) | (((row >> 3) & 1) << 1)) : ((row & 3) | (((row >> 3) & 1) << 2)); }
};

template <int BM, int BN, int KTOT, int N, int WM, int WN, int NS, int SUB, int PD, int EPI, int MAP, bool STI>
DI void gemm_body(const GemmArgs& p) {
  constexpr int NK = KTOT / (64 * SUB);
  constexpr int ASTAGE = BM * 128;
  constexpr int BSTAGE = BL<BN>::STAGE;
  constexpr int SSTAGE = ASTAGE + BSTAGE;
  constexpr int STAGE = SSTAGE * SUB;
  constexpr int MF = BM / WM / 16, NF = BN / WN / 16;
  static_assert(WM * WN == 8, "8 compute waves");
  constexpr int LA = BM / 32;
  constexpr int LB = BL<BN>::NI / 4;
  constexpr int L = (LA + LB) * SUB;
  constexpr int D = NS - 1;
  constexpr bool LN = (EPI != EPI_RES);
  constexpr int XOFF = NS * STAGE;
  constexpr int EOFF = XOFF + 2048;
  constexpr int NE = (EPI == EPI_RES) ? 2 : (STI ? 0 : (EPI == EPI_FFN1 ? 5 : 4));
  constexpr int SMEM = EOFF + ((EPI == EPI_RES) ? 8192 : (STI ? 0 : 16384 + 1024));
  __shared__ __attribute__((aligned(256))) char smem[SMEM];
  const unsigned lds0 = (unsigned)(size_t)(LDSP(char))smem;
  float* csb = (float*)(smem + XOFF);
  float* st = csb + 256;

  const int tid = threadIdx.x, lane = tid & 63;
  const int wid = __builtin_amdgcn_readfirstlane(tid >> 6);
  const int l15 = lane & 15, g = lane >> 4;

  constexpr int GM = SEQ / BM, GN = N / BN;
  int pm, pn;
  {
    const int bid = blockIdx.x, xcd = bid & 7, idx = bid >> 3;
    if (MAP == 0) {
      constexpr int PER = GM / 8;
      pm = xcd * PER + idx / GN; pn = idx % GN;
    } else {
      constexpr int PM = GM / 4, PN = GN / 2;
      pm = (xcd >> 1) * PM + idx / PN; pn = (xcd & 1) * PN + idx % PN;
    }
  }
  const int m0 = pm * BM, n0 = pn * BN;
  const int bq = l15 >> 2, bp = lane & 3;
  auto bfrag_off = [&](int f) -> unsigned {
    if (BL<BN>::PADDED) return (2 * g) * BL<BN>::GS + bq * BL<BN>::RB + f * 32 + bp * 8;
    const int row = 8 * g + bq;
    return row * BL<BN>::RB + ((f ^ BL<BN>::sw(row)) * 32) + bp * 8;
  };

  if (wid >= 8) {
    const int w4 = wid - 8;
    __builtin_amdgcn_s_setprio(3);
    unsigned aoff[LA], boff[LB];
#pragma unroll
    for (int i = 0; i < LA; ++i) {
      const int ins = w4 * LA + i;
      const int row = ins * 8 + (lane >> 3), ch = (lane & 7) ^ ((row >> 1) & 7);
      aoff[i] = (unsigned)(row * KTOT * 2 + ch * 16);
    }
#pragma unroll
    for (int i = 0; i < LB; ++i) {
      const int ins = w4 * LB + i;
      constexpr int CPR = BN / 8;
      if (BL<BN>::PADDED) {
        const int row = ins * 4 + lane / CPR, ch = lane % CPR;
        boff[i] = (unsigned)(row * N * 2 + ch * 16);
      } else {
        const int row = ins * (64 / CPR) + lane / CPR;
        const int ch = (lane % CPR) ^ (BL<BN>::sw(row) << 1);
        boff[i] = (unsigned)(row * N * 2 + ch * 16);
      }
    }
    const char* abase = (const char*)p.A + (size_t)m0 * KTOT * 2;
    const char* bbase = (const char*)p.B + (size_t)n0 * 2;
    auto issue = [&](int s) {
      if (ABL & 1) return;
#pragma unroll
      for (int u = 0; u < SUB; ++u) {
        const int ss = s * SUB + u;
        const unsigned slot = lds0 + (s % NS) * STAGE + u * SSTAGE;
#pragma unroll
        for (int i = 0; i < LA; ++i) glds16(abase + ss * 128, aoff[i], slot + (w4 * LA + i) * 1024);
#pragma unroll
        for (int i = 0; i < LB; ++i) {
          if (BL<BN>::PADDED) glds16m<BL<BN>::PADDED ? 4 * (BN / 8) : 64>(bbase + (size_t)ss * 64 * N * 2, boff[i], slot + ASTAGE + (w4 * LB + i) * BL<BN>::GS);
          else glds16(bbase + (size_t)ss * 64 * N * 2, boff[i], slot + ASTAGE + (w4 * LB + i) * 1024);
        }
      }
    };
    auto extras = [&]() {
      if (ABL & 1) return;
      if (EPI == EPI_RES) {
#pragma unroll
        for (int i = 0; i < 2; ++i)
        {
          const int row = (w4 * 2 + i) * 8 + (lane >> 3), ch = (lane & 7) ^ ((row >> 1) & 7);
          glds16((const char*)p.aux + ((size_t)m0 * N + n0) * 2, (unsigned)(row * N * 2 + ch * 16), lds0 + EOFF + (w4 * 2 + i) * 1024);
        }
      } else if (!STI) {
#pragma unroll
        for (int i = 0; i < 4; ++i) glds16((const char*)p.stats + (size_t)m0 * 128 + (w4 * 4 + i) * 1024, lane * 16, lds0 + EOFF + (w4 * 4 + i) * 1024);
        if (EPI == EPI_FFN1) glds16m<32>((const char*)p.aux + n0 * 2, (lane & 15) * 16, lds0 + EOFF + 16384);
      }
    };
    auto upto = [](int j) { const int a = 2 + 2 * j, b = j + D; const int c = a < b ? a : b; return c < NK ? c : NK; };
#pragma unroll
    for (int s = 0; s < upto(0); ++s) issue(s);
    if (upto(0) == NK) extras();
    const bf16x8 ONES = __builtin_bit_cast(bf16x8, u32x4{0x3f803f80u, 0x3f803f80u, 0x3f803f80u, 0x3f803f80u});
    const unsigned c_rd0 = lds0 + ASTAGE + bfrag_off(w4), c_rd1 = lds0 + ASTAGE + bfrag_off(w4 + 4);
    const unsigned a_rd = lds0 + (w4 * 32 + l15) * 128 + ((g ^ (l15 >> 1)) * 16);
    f32x4 cs0 = {0.f, 0.f, 0.f, 0.f}, cs1 = cs0, rs[2] = {cs0, cs0}, rq[2] = {cs0, cs0};
#pragma unroll
    for (int j = 0; j < NK; ++j) {
      const int behind = (upto(j) - 1 - j) * L + (upto(j) == NK ? NE : 0);
      wait_bar_n(behind);
#pragma unroll
      for (int s = upto(j); s < upto(j + 1); ++s) issue(s);
      if (upto(j) < NK && upto(j + 1) == NK) extras();
      if (LN && !(ABL & 16)) {
        const unsigned so = (j % NS) * STAGE;
#pragma unroll
        for (int ks = 0; ks < 2; ++ks) {
          const unsigned o = so + ks * 8 * BL<BN>::GS;
          cs0 = __builtin_amdgcn_mfma_f32_16x16x32_bf16(CAT8(trread(c_rd0 + o), trread(c_rd0 + o + BL<BN>::GS)), ONES, cs0, 0, 0, 0);
          cs1 = __builtin_amdgcn_mfma_f32_16x16x32_bf16(CAT8(trread(c_rd1 + o), trread(c_rd1 + o + BL<BN>::GS)), ONES, cs1, 0, 0, 0);
          if (STI) {
#pragma unroll
            for (int mf = 0; mf < 2; ++mf) {
              const bf16x8 a = ldsread16((a_rd ^ (ks * 64)) + so + mf * 2048);
              rs[mf] = __builtin_amdgcn_mfma_f32_16x16x32_bf16(ONES, a, rs[mf], 0, 0, 0);
              rq[mf] = __builtin_amdgcn_mfma_f32_16x16x32_bf16(a, a, rq[mf], 0, 0, 0);
            }
          }
        }
        PIN(cs0); PIN(cs1);
        if (STI) { PIN(rs[0]); PIN(rs[1]); PIN(rq[0]); PIN(rq[1]); }
      }
    }
    if (LN) {
      if (l15 == 0) {
        *(f32x4*)(csb + w4 * 16 + g * 4) = cs0;
        *(f32x4*)(csb + 64 + w4 * 16 + g * 4) = cs1;
      }
      if (STI) {
#pragma unroll
        for (int mf = 0; mf < 2; ++mf) {
          const int jj = l15 & 3;
          const float d = (jj == 0) ? rq[mf][0] : (jj == 1) ? rq[mf][1] : (jj == 2) ? rq[mf][2] : rq[mf][3];
          if (g == (l15 >> 2)) *(f32x2*)(st + (w4 * 32 + mf * 16 + l15) * 2) = f32x2{rs[mf][0], d};
        }
      } else {
        wait_vm<0>();
#pragma unroll
        for (int i = 0; i < 4; ++i) {
          const f32x4 v = *(const f32x4*)(smem + EOFF + (w4 * 4 + i) * 1024 + lane * 16);
          float a = v[0] + v[2], b = v[1] + v[3];
          a += dppf<0xB1>(a); b += dppf<0xB1>(b); a += dppf<0x4E>(a); b += dppf<0x4E>(b); a += dppf<0x141>(a); b += dppf<0x141>(b);
          if ((lane & 7) == 0) *(f32x2*)(st + (w4 * 32 + i * 8 + (lane >> 3)) * 2) = f32x2{a, b};
        }
      }
    }
    wait_bar<0>();
    return;
  }

  const int wm = wid % WM, wn = wid / WM;
  const int trow = m0 + wm * (MF * 16) + l15;
  const int ncol = n0 + wn * NF * 16;
  u32x2 pre[MF][NF];
  float rstd[MF], nmr[MF];
  auto mkstats = [&](int mf, float s1, float s2) {
    const float mean = s1 * (1.f / KTOT);
    const float var = fmaxf(s2 * (1.f / KTOT) - mean * mean, 0.f);
    rstd[mf] = __builtin_amdgcn_rsqf(var + 1e-5f);
    nmr[mf] = -mean * rstd[mf];
  };
  const unsigned a_rd = lds0 + (wm * (MF * 16) + l15) * 128 + ((g ^ (l15 >> 1)) * 16);
  unsigned b_rd[BL<BN>::PADDED ? 1 : NF];
#pragma unroll
  for (int nf = 0; nf < (BL<BN>::PADDED ? 1 : NF); ++nf) b_rd[nf] = lds0 + ASTAGE + bfrag_off(wn * NF + nf);
  f32x4 acc[MF][NF];
#pragma unroll
  for (int a = 0; a < MF; ++a)
#pragma unroll
    for (int b = 0; b < NF; ++b) acc[a][b] = f32x4{0.f, 0.f, 0.f, 0.f};

  constexpr int KS = 2 * SUB, GPS = KS / PD, NG = NK * GPS;
  bf16x8 af[2][PD][MF], bfr[2][PD][NF];
  auto rdfrags = [&](int gi) {
    if ((ABL & 2) && gi > 1) return;
    const int pb = gi & 1, s = gi / GPS;
#pragma unroll
    for (int q = 0; q < PD; ++q) {
      const int k = (gi % GPS) * PD + q;
      const unsigned so = (s % NS) * STAGE + (k >> 1) * SSTAGE;
#pragma unroll
      for (int mf = 0; mf < MF; ++mf) af[pb][q][mf] = ldsread16((a_rd ^ ((k & 1) * 64)) + so + mf * 2048);
#pragma unroll
      for (int nf = 0; nf < NF; ++nf) {
        const unsigned o = (BL<BN>::PADDED ? b_rd[0] + nf * 32 : b_rd[nf]) + so + (k & 1) * 8 * BL<BN>::GS;
        bfr[pb][q][nf] = CAT8(trread(o), trread(o + BL<BN>::GS));
      }
    }
  };
  unsigned codev = 0;
  if (EPI == EPI_QKV && STI && !NOCP) {
    unsigned long long lo, hi;
    asm volatile("s_getpc_b64 s[20:21]\n\ts_add_u32 s20, s20, k_attn@rel32@lo+4\n\ts_addc_u32 s21, s21, k_attn@rel32@hi+12\n\ts_mov_b64 %0, s[20:21]\n\t"
                 "s_getpc_b64 s[20:21]\n\ts_add_u32 s20, s20, k_zend@rel32@lo+4\n\ts_addc_u32 s21, s21, k_zend@rel32@hi+12\n\ts_mov_b64 %1, s[20:21]"
                 : "=s"(lo), "=s"(hi)::"s20", "s21", "scc");
    const unsigned long long a = lo + (unsigned long long)(((blockIdx.x >> 3) * 8 + wid) * 256 + lane * 4) ;
    if (a + 4 <= hi) codev = *(const __attribute__((address_space(1))) unsigned*)a;
  }
  lds_bar();
  rdfrags(0);
#pragma unroll
  for (int gi = 0; gi < NG; ++gi) {
    if (gi + 1 < NG) {
      if ((gi + 1) % GPS == 0) {
        lds_bar();
      }
      SBAR();
      rdfrags(gi + 1);
    }
    const int pb = gi & 1;
    if (!(ABL & 8) || gi < 2)
#pragma unroll
    for (int q = 0; q < PD; ++q)
#pragma unroll
      for (int nf = 0; nf < NF; ++nf)
#pragma unroll
        for (int mf = 0; mf < MF; ++mf)
          acc[mf][nf] = __builtin_amdgcn_mfma_f32_16x16x32_bf16(bfr[pb][q][nf], af[pb][q][mf], acc[mf][nf], 0, 0, 0);
    if (gi + 1 < NG) {
      constexpr int NM = PD * MF * NF, ND = PD * (MF + 2 * NF);
      constexpr int PER = (ND + NM - 1) / NM;
#pragma unroll
      for (int i = 0; i < NM; ++i) {
        __builtin_amdgcn_sched_group_barrier(0x100, PER, 0);
        __builtin_amdgcn_sched_group_barrier(0x8, 1, 0);
      }
    }
#pragma unroll
    for (int mf = 0; mf < MF; ++mf)
#pragma unroll
      for (int nf = 0; nf < NF; ++nf) PIN(acc[mf][nf]);
  }
  if (EPI == EPI_QKV && STI) asm volatile("" ::"v"(codev));
  lds_bar();
  if (ABL & 4) { if (acc[0][0][0] == 123.456f) p.out0[0] = 1; return; }
  if (LN) {
    f32x4 c0[NF];
#pragma unroll
    for (int nf = 0; nf < NF; ++nf) c0[nf] = *(const f32x4*)(csb + (wn * NF + nf) * 16 + g * 4);
#pragma unroll
    for (int mf = 0; mf < MF; ++mf) { const f32x2 a = *(const f32x2*)(st + (wm * (MF * 16) + mf * 16 + l15) * 2); mkstats(mf, a[0], a[1]); }
    if (EPI == EPI_FFN1) {
#pragma unroll
      for (int nf = 0; nf < NF; ++nf) pre[0][nf] = *(const u32x2*)(smem + EOFF + 16384 + ((wn * NF + nf) * 16 + g * 4) * 2);
    }
    if (EPI == EPI_QKV) {
      const int tk = trow + (g & 1) * 16;
      const unsigned lin = tk * 16 + (g >> 1) * 8;
      const unsigned kof = (tk >> 5) * 512 + (g >> 1) * 256 + (tk & 31) * 8;
#pragma unroll
      for (int nf = 0; nf < NF; ++nf) {
        const int hh = (ncol >> 4) + nf;
        const bool isq = hh < 32, isk = (hh >> 5) == 1;
        const float sc = isq ? 0.25f * 1.4426950408889634f : 1.f;
        u16* base = p.out0 + (size_t)hh * (SEQ * 16);
        u32x2 o[MF];
#pragma unroll
        for (int mf = 0; mf < MF; ++mf) {
          acc[mf][nf] = acc[mf][nf] * (rstd[mf] * sc) + c0[nf] * (nmr[mf] * sc);
          const f32x4 y = acc[mf][nf];
          o[mf] = u32x2{cvtpk(y[0], y[1]), cvtpk(y[2], y[3])};
        }
        st16(base + (isk ? kof : lin), pair16(o[0], o[MF - 1]));
      }
    } else {
      u32x2 o[MF][NF];
#pragma unroll
      for (int nf = 0; nf < NF; ++nf) {
        const f32x4 bias = {bflo(pre[0][nf][0]), bfhi(pre[0][nf][0]), bflo(pre[0][nf][1]), bfhi(pre[0][nf][1])};
#pragma unroll
        for (int mf = 0; mf < MF; ++mf) {
          const f32x4 h = acc[mf][nf] * rstd[mf] + (c0[nf] * nmr[mf] + bias);
          const f32x4 t = h * 1.4426950408889634f;
          f32x4 u, rc;
#pragma unroll
          for (int j = 0; j < 4; ++j) u[j] = __builtin_amdgcn_exp2f(t[j]);
          const f32x4 d = u * (u * -0.5f - 1.f) - 1.f;
#pragma unroll
          for (int j = 0; j < 4; ++j) rc[j] = __builtin_amdgcn_rcpf(d[j]);
          const f32x4 a = h * rc + h;
          o[mf][nf] = u32x2{cvtpk(a[0], a[1]), cvtpk(a[2], a[3])};
          if (nf & 1) st16pair(p.out0 + (size_t)(trow + mf * 16) * N + ncol + (nf - 1) * 16, g, o[mf][nf - 1], o[mf][nf]);
        }
      }
    }
  } else {
#pragma unroll
    for (int mf = 0; mf < MF; ++mf) {
      const int tok = trow + mf * 16;
      float s1 = 0.f, s2 = 0.f;
      u32x2 o[NF];
#pragma unroll
      for (int nf = 0; nf < NF; ++nf) {
        const u32x2 rr = *(const u32x2*)(smem + EOFF + (wm * (MF * 16) + mf * 16 + l15) * 128 + ((((wn * NF + nf) * 2 + (g >> 1)) ^ (l15 >> 1)) * 16) + (g & 1) * 8);
        const f32x4 r = acc[mf][nf];
        o[nf] = u32x2{cvtpk(r[0] + bflo(rr[0]), r[1] + bfhi(rr[0])), cvtpk(r[2] + bflo(rr[1]), r[3] + bfhi(rr[1]))};
        const float e0 = bflo(o[nf][0]), e1 = bfhi(o[nf][0]), e2 = bflo(o[nf][1]), e3 = bfhi(o[nf][1]);
        s1 += (e0 + e1) + (e2 + e3);
        s2 += (e0 * e0 + e1 * e1) + (e2 * e2 + e3 * e3);
        if (nf & 1) st16pair(p.out0 + (size_t)tok * N + ncol + (nf - 1) * 16, g, o[nf - 1], o[nf]);
      }
      if (STI) {
        s1 = sum32(sum16(s1)); s2 = sum32(sum16(s2));
        if (g == 0) *(f32x2*)(p.stats + tok * 32 + (ncol >> 5) * 2) = f32x2{s1, s2};
      }
    }
  }
}

constexpr int AT_K = 0, AT_V = 65536, AT_ONE = 131072, AT_X = AT_ONE + 6144, AT_Q = AT_X + 256, AT_PF = AT_Q + 8 * 9 * 256, AT_BYTES = AT_PF + 1024;

#define ATT_HALF(SC, OO, VC, MID)                                                                            \
  {                                                                                                          \
    _Pragma("unroll") for (int i = 0; i < 4; ++i) SC[i] = __builtin_amdgcn_exp2f(SC[i]);                     \
    SBAR(); MID; SBAR();                                                                                     \
    _Pragma("unroll") for (int i = 4; i < 9; ++i) SC[i] = __builtin_amdgcn_exp2f(SC[i]);                     \
    u32x4 p0 = {cvtpk(SC[0], SC[1]), cvtpk(SC[2], SC[3]), cvtpk(SC[4], SC[5]), cvtpk(SC[6], SC[7])};         \
    PIN(p0); SBAR();                                                                                         \
    SC[9] = __builtin_amdgcn_exp2f(SC[9]); SBAR();                                                           \
    OO = MFMA32(CAT8(VC[0], VC[1]), __builtin_bit_cast(bf16x8, p0), OO); SBAR();                             \
    _Pragma("unroll") for (int i = 10; i < 16; ++i) SC[i] = __builtin_amdgcn_exp2f(SC[i]);                   \
    u32x4 p1 = {cvtpk(SC[8], SC[9]), cvtpk(SC[10], SC[11]), cvtpk(SC[12], SC[13]), cvtpk(SC[14], SC[15])};   \
    OO = MFMA32(CAT8(VC[2], VC[3]), __builtin_bit_cast(bf16x8, p1), OO); SBAR();                             \
  }
#define ATT_TILE(VC, VN, T)                                                                                  \
  ATT_HALF(s0, o0, VC, { s1 = MFMA32(kf, qf1, cm1); PIN(s1); kf = ldsread16(kaddr + ((T) + 1) * 1024);       \
       VN[0] = trread(vaddr + ((T) + 1) * 1024); VN[1] = trread(vaddr + ((T) + 1) * 1024 + 256); })          \
  ATT_HALF(s1, o1, VC, { s0 = MFMA32(kf, qf0, cm0); PIN(s0);                                                 \
       VN[2] = trread(vaddr + ((T) + 1) * 1024 + 512); VN[3] = trread(vaddr + ((T) + 1) * 1024 + 768); })

template <bool FAST>
DI void attn_pass(unsigned lds0, int lane, int ks, const bf16x8 qf0, const bf16x8 qf1, float nm0, float nm1, f32x16& o0, f32x16& o1,
                  const char* kb, const char* vb, unsigned kvdst, const char* pf, unsigned pfoff, unsigned pfstep) {
  const int h = lane >> 5;
  unsigned kaddr = lds0 + AT_K + ks * 32768 + h * 512 + (lane & 31) * 16;
  const bool cl = (lane >> 4) & 1;
  const int qq = (lane & 15) >> 2, pp = lane & 3;
  unsigned vaddr = cl ? lds0 + AT_ONE + (4 * (h ^ 1) + qq) * 32 + pp * 8 : lds0 + AT_V + ks * 32768 + (4 * h + qq) * 32 + pp * 8;
  const unsigned vstep = cl ? 0u : 4096u;
  f32x16 cm0, cm1, s0, s1;
  s16x4 vA[4], vB[4];
#pragma unroll
  for (int i = 0; i < 16; ++i) { cm0[i] = FAST ? 0.f : nm0; cm1[i] = FAST ? 0.f : nm1; o0[i] = 0.f; o1[i] = 0.f; }
  bf16x8 kf = ldsread16(kaddr);
  s0 = MFMA32(kf, qf0, cm0);
  vA[0] = trread(vaddr); vA[1] = trread(vaddr + 256); vA[2] = trread(vaddr + 512); vA[3] = trread(vaddr + 768);
  for (int it = 0; it < 8; ++it) {
    PIN(kaddr); PIN(vaddr);
    if (FAST && it < 6) {
      glds16(kb + (it + 2) * 4096, lane * 16, kvdst + AT_K + (it + 2) * 4096);
      glds16(vb + (it + 2) * 4096, lane * 16, kvdst + AT_V + (it + 2) * 4096);
    }
    if (FAST) { glds16(pf, pfoff, lds0 + AT_PF); pfoff += pfstep; }
    ATT_TILE(vA, vB, 0) ATT_TILE(vB, vA, 1) ATT_TILE(vA, vB, 2) ATT_TILE(vB, vA, 3)
    kaddr += 4096; vaddr += vstep;
    if (FAST) wait_bar<1>(); else wait_bar<0>();
  }
}

extern "C" __global__ __launch_bounds__(512, 2) void k_attn(const u16* __restrict__ Q, const u16* __restrict__ K, const u16* __restrict__ V,
                                                  u16* __restrict__ O,
                                                  const u16* __restrict__ wo, const u16* __restrict__ w1, const u16* __restrict__ w2) {
  __shared__ __attribute__((aligned(256))) char smem[AT_BYTES];
  const unsigned lds0 = (unsigned)(size_t)(LDSP(char))smem;
  const int tid = threadIdx.x, lane = tid & 63;
  const int wid = __builtin_amdgcn_readfirstlane(tid >> 6);
  const int wq = wid & 3, ks = wid >> 2;
  const int bid = blockIdx.x;
  const int head = (bid & 7) * 4 + ((bid >> 3) & 3), qb = bid >> 5;
  const unsigned l16 = lane * 16;
  const char* kb = (const char*)K + (size_t)head * 65536 + (ks * 32 + wq) * 1024;
  const char* vb = (const char*)V + (size_t)head * 65536 + (ks * 32 + wq) * 1024;
  const unsigned kvdst = lds0 + (ks * 32 + wq) * 1024;
  glds16((const char*)Q + ((size_t)head * SEQ + qb * 256 + wid * 32) * 32, l16, lds0 + AT_Q + wid * 1024);
#pragma unroll
  for (int i = 0; i < 2; ++i) {
    glds16(kb + i * 4096, l16, kvdst + AT_K + i * 4096);
    glds16(vb + i * 4096, l16, kvdst + AT_V + i * 4096);
  }
  if (tid < 384) *(u32x4*)(smem + AT_ONE + tid * 16) = u32x4{(tid & 1) ? 0u : 0x3f80u, 0u, 0u, 0u};
  if (tid == 511) *(unsigned*)(smem + AT_X) = 0u;
  wait_bar<0>();
  const bf16x8 qf0 = ldsread16(lds0 + AT_Q + wq * 2048 + (lane & 31) * 32 + (lane >> 5) * 16);
  const bf16x8 qf1 = ldsread16(lds0 + AT_Q + wq * 2048 + 1024 + (lane & 31) * 32 + (lane >> 5) * 16);
  const char* pf; unsigned pfoff, pfstep;
  {
    const int u = (bid >> 3) * 8 + wid, half = bid & 1;
    if (u < 32) { pf = (const char*)wo + half * 512; pfoff = (u * 16 + (lane >> 5)) * 1024 + (lane & 31) * 16; pfstep = 2048; }
    else if (u < 144) { pf = (const char*)w2 + half * 512; pfoff = ((u - 32) * 16 + (lane >> 5)) * 1024 + (lane & 31) * 16; pfstep = 2048; }
    else {
      const unsigned c0 = (u - 144) * 64 + lane;
      pf = (const char*)w1 + half * 1792; pfoff = (c0 / 112) * 3584 + (c0 % 112) * 16; pfstep = 64 * 3584;
    }
  }
  f32x16 o0, o1;
  attn_pass<true>(lds0, lane, ks, qf0, qf1, 0.f, 0.f, o0, o1, kb, vb, kvdst, pf, pfoff, pfstep);
  wait_vm<0>();
  float* xb = (float*)(smem + AT_Q);
  if (lane < 32 && !(o0[8] >= 8.67e-19f && o0[8] <= 1.26e30f && o1[8] >= 8.67e-19f && o1[8] <= 1.26e30f)) *(unsigned*)(smem + AT_X) = 1u;
#pragma unroll
  for (int i = 0; i < 9; ++i) xb[(wid * 9 + i) * 64 + lane] = ks ? o0[i] : o1[i];
  lds_bar();
  if (__builtin_amdgcn_readfirstlane(*(const volatile unsigned*)(smem + AT_X)) != 0u) {
    float m0 = -3.0e38f, m1 = -3.0e38f;
    const unsigned kaddr = lds0 + AT_K + (lane >> 5) * 512 + (lane & 31) * 16;
    f32x16 z;
#pragma unroll
    for (int i = 0; i < 16; ++i) z[i] = 0.f;
    for (int t = 0; t < 64; ++t) {
      const bf16x8 kf = ldsread16(kaddr + t * 1024);
      const f32x16 sa = MFMA32(kf, qf0, z), sb = MFMA32(kf, qf1, z);
#pragma unroll
      for (int i = 0; i < 16; ++i) { m0 = fmaxf(m0, sa[i]); m1 = fmaxf(m1, sb[i]); }
    }
    m0 = max32(m0); m1 = max32(m1);
    attn_pass<false>(lds0, lane, ks, qf0, qf1, -m0, -m1, o0, o1, kb, vb, kvdst, pf, pfoff, pfstep);
#pragma unroll
    for (int i = 0; i < 9; ++i) xb[(wid * 9 + i) * 64 + lane] = ks ? o0[i] : o1[i];
    lds_bar();
  }
  f32x16 o = ks ? o1 : o0;
#pragma unroll
  for (int i = 0; i < 9; ++i) o[i] += xb[((wid ^ 4) * 9 + i) * 64 + lane];
  auto lr = __builtin_amdgcn_permlane32_swap(__float_as_uint(o[8]), __float_as_uint(o[8]), false, false);
  const float rl = __builtin_amdgcn_rcpf(__uint_as_float(lr[0]));
  unsigned x0 = cvtpk(o[0] * rl, o[1] * rl), x1 = cvtpk(o[2] * rl, o[3] * rl);
  unsigned y0 = cvtpk(o[4] * rl, o[5] * rl), y1 = cvtpk(o[6] * rl, o[7] * rl);
  auto r0 = __builtin_amdgcn_permlane32_swap(x0, y0, false, false);
  auto r1 = __builtin_amdgcn_permlane32_swap(x1, y1, false, false);
  u32x4 ov = {r0[0], r1[0], r0[1], r1[1]};
  const int q0 = qb * 256 + wq * 64 + ks * 32;
  *(u32x4*)(O + (size_t)(q0 + (lane & 31)) * DM + head * 16 + (lane >> 5) * 8) = ov;
}

#define QNS 3
#define QPD 2
#define RNS 3
#define RPD 2
#define MQ 1
#define MO 1
#define M1 1
#define M2 1
#define GEMM_KERNEL(name, ...) extern "C" __global__ __launch_bounds__(768, 3) void name(GemmArgs p) { gemm_body<__VA_ARGS__>(p); }
GEMM_KERNEL(k_oproj, 64, 64, 512, 512, 4, 2, RNS, 2, RPD, EPI_RES, MO, true)
GEMM_KERNEL(k_ffn1, 128, 128, 512, 1792, 4, 2, QNS, 1, QPD, EPI_FFN1, M1, false)
GEMM_KERNEL(k_ffn2, 64, 64, 1792, 512, 4, 2, RNS, 2, RPD, EPI_RES, M2, true)
GEMM_KERNEL(k_qkv2, 128, 96, 512, 1536, 4, 2, QNS, 1, QPD, EPI_QKV, MQ, false)
extern "C" __global__ void k_zend() {}
GEMM_KERNEL(k_qkv1, 128, 96, 512, 1536, 4, 2, QNS, 1, QPD, EPI_QKV, MQ, true)

extern "C" void kernel_launch(void* const* d_in, const int* in_sizes, int n_in, void* d_out, int out_size, void* d_ws,
                              size_t ws_size, hipStream_t stream) {
  const u16* x = (const u16*)d_in[0];
  const u16* wqkv = (const u16*)d_in[1];
  const u16* wo = (const u16*)d_in[2];
  const u16* w1 = (const u16*)d_in[4];
  const u16* b1 = (const u16*)d_in[5];
  const u16* w2 = (const u16*)d_in[6];
  char* ws = (char*)d_ws;
  const size_t MB = 1 << 20;
  u16* Qb = (u16*)(ws + 0 * MB);
  u16* Kb = (u16*)(ws + 2 * MB);
  u16* Vb = (u16*)(ws + 4 * MB);
  u16* Ob = (u16*)(ws + 6 * MB);
  u16* X1 = (u16*)(ws + 8 * MB);
  u16* X2 = (u16*)(ws + 10 * MB);
  u16* Hb = (u16*)(ws + 12 * MB);
  float* kmax = (float*)(ws + 20 * MB);
  float* st1 = (float*)(ws + 21 * MB);
  float* st2 = (float*)(ws + 22 * MB);
  const u16* xin = x;
  for (int layer = 0; layer < 2; ++layer) {
    u16* xout = (layer == 0) ? X2 : (u16*)d_out;
    GemmArgs a;
    a = GemmArgs{xin, wqkv, nullptr, Qb, Kb, Vb, kmax, st2};
    if (layer == 0) k_qkv1<<<256, 768, 0, stream>>>(a);
    else k_qkv2<<<256, 768, 0, stream>>>(a);
    k_attn<<<256, 512, 0, stream>>>(Qb, Kb, Vb, Ob, wo, w1, w2);
    a = GemmArgs{Ob, wo, xin, X1, nullptr, nullptr, nullptr, st1};
    k_oproj<<<256, 768, 0, stream>>>(a);
    a = GemmArgs{X1, w1, b1, Hb, nullptr, nullptr, nullptr, st1};
    k_ffn1<<<224, 768, 0, stream>>>(a);
    a = GemmArgs{Hb, w2, X1, xout, nullptr, nullptr, nullptr, st2};
    k_ffn2<<<256, 768, 0, stream>>>(a);
    xin = X2;
  }
}
```

```cpp
#include <hip/hip_runtime.h>
#include <hip/hip_bf16.h>
#include <stdint.h>

typedef short bf16x8 __attribute__((ext_vector_type(8)));
typedef short s16x4 __attribute__((ext_vector_type(4)));
typedef __bf16 bf16x2 __attribute__((ext_vector_type(2)));
typedef float f32x4 __attribute__((ext_vector_type(4)));
typedef float f32x16 __attribute__((ext_vector_type(16)));
typedef unsigned u32x2 __attribute__((ext_vector_type(2)));
typedef unsigned u32x4 __attribute__((ext_vector_type(4)));
typedef unsigned short u16;
#define LDSP(T) __attribute__((address_space(3))) T*
#define DI __device__ __forceinline__

#define NOCP 0
#define POLLSLEEP 8
#define EARLYB 0
#define PB_O 3
#define PB_1 3
#define PB_2 3
#define PB_Q 4
#define ABL 0
constexpr int SEQ = 2048, DM = 512, NH = 32, DH = 16, FF = 1792;

typedef float f32x2 __attribute__((ext_vector_type(2)));
DI unsigned cvtpk(float lo, float hi) { return __builtin_bit_cast(unsigned, __builtin_convertvector(f32x2{lo, hi}, bf16x2)); }
DI float bflo(unsigned u) { return __uint_as_float(u << 16); }
DI float bfhi(unsigned u) { return __uint_as_float(u & 0xffff0000u); }
DI float dot2(unsigned a, unsigned b, float c) {
  return __builtin_amdgcn_fdot2_f32_bf16(__builtin_bit_cast(bf16x2, a), __builtin_bit_cast(bf16x2, b), c, false);
}
DI void glds16(const void* sbase, unsigned voff, unsigned lds_addr) {
  unsigned sv;
  asm volatile("s_mov_b32 %0, m0\n\ts_mov_b32 m0, %3\n\ts_nop 0\n\tglobal_load_lds_dwordx4 %1, %2\n\ts_mov_b32 m0, %0"
               : "=&s"(sv) : "v"(voff), "s"(sbase), "s"(lds_addr) : "memory");
}
DI void glds16s(const void* sbase, unsigned voff, unsigned lds_addr) {
  unsigned sv;
  asm volatile("s_mov_b32 %0, m0\n\ts_mov_b32 m0, %3\n\ts_nop 0\n\tglobal_load_lds_dwordx4 %1, %2 sc1\n\ts_mov_b32 m0, %0"
               : "=&s"(sv) : "v"(voff), "s"(sbase), "s"(lds_addr) : "memory");
}
template <int NL> DI void glds16m(const void* sbase, unsigned voff, unsigned lds_addr) {
  unsigned sv;
  asm volatile("s_mov_b32 %0, m0\n\ts_mov_b32 m0, %3\n\ts_mov_b32 exec_hi, %4\n\tglobal_load_lds_dwordx4 %1, %2\n\ts_mov_b32 exec_hi, -1\n\ts_mov_b32 m0, %0"
               : "=&s"(sv) : "v"(voff), "s"(sbase), "s"(lds_addr), "i"((unsigned)((1ull << (NL - 32)) - 1)) : "memory");
}
#define SBAR() __builtin_amdgcn_sched_barrier(0)
#define PIN(x) asm volatile("" : "+v"(x))
#define MFMA32(a, b, c) __builtin_amdgcn_mfma_f32_32x32x16_bf16(a, b, c, 0, 0, 0)
#define CAT8(a, b) __builtin_shufflevector(a, b, 0, 1, 2, 3, 4, 5, 6, 7)
template <int N> DI void wait_bar() { asm volatile("s_waitcnt vmcnt(%0) lgkmcnt(0)\n\ts_barrier" ::"i"(N) : "memory"); }
DI void wait_bar_n(int n) { asm volatile("s_waitcnt vmcnt(%0) lgkmcnt(0)\n\ts_barrier" ::"i"(n) : "memory"); }
template <int N> DI void wait_vm() { asm volatile("s_waitcnt vmcnt(%0)" ::"i"(N) : "memory"); }
DI void lds_bar() { asm volatile("s_waitcnt lgkmcnt(0)\n\ts_barrier" ::: "memory"); }
#define STF 1
DI void st16(void* ptr, u32x4 v) {
  if (ABL & 32) { PIN(v); return; }
  if (STF == 0) *(u32x4*)ptr = v;
  else asm volatile("global_store_dwordx4 %0, %1, off sc1\n\ts_nop 1" ::"v"(ptr), "v"(v) : "memory");
}
DI u32x4 pair16(u32x2 a, u32x2 b) {
  auto r0 = __builtin_amdgcn_permlane16_swap(a[0], b[0], false, false);
  auto r1 = __builtin_amdgcn_permlane16_swap(a[1], b[1], false, false);
  return u32x4{r0[0], r1[0], r0[1], r1[1]};
}
DI void st16pair(u16* rowp, int g, u32x2 a, u32x2 b) { st16(rowp + (g & 1) * 16 + (g >> 1) * 8, pair16(a, b)); }
DI void st8(void* ptr, f32x2 v) { asm volatile("global_store_dwordx2 %0, %1, off sc1" ::"v"(ptr), "v"(v) : "memory"); }
typedef __attribute__((address_space(1))) unsigned gu32;
DI void wait_cnt(unsigned* c, unsigned target, int lane) {
  for (unsigned spins = 0; spins < 200000u; ++spins) {
    unsigned v = 0;
    if (lane == 0) v = __hip_atomic_load((gu32*)c, __ATOMIC_RELAXED, __HIP_MEMORY_SCOPE_AGENT);
    if ((unsigned)__builtin_amdgcn_readfirstlane(v) >= target) break;
    if (POLLSLEEP) __builtin_amdgcn_s_sleep(POLLSLEEP);
  }
  asm volatile("" ::: "memory");
}
DI s16x4 trread(unsigned addr) {
  return __builtin_bit_cast(s16x4, __builtin_amdgcn_ds_read_tr16_b64_v4i16((LDSP(s16x4))(size_t)addr));
}
DI bf16x8 ldsread16(unsigned addr) { return *(const LDSP(bf16x8))(size_t)addr; }
DI float sum16(float v) { auto r = __builtin_amdgcn_permlane16_swap(__float_as_uint(v), __float_as_uint(v), false, false); return __uint_as_float(r[0]) + __uint_as_float(r[1]); }
DI float sum32(float v) { auto r = __builtin_amdgcn_permlane32_swap(__float_as_uint(v), __float_as_uint(v), false, false); return __uint_as_float(r[0]) + __uint_as_float(r[1]); }
DI float max16(float v) { auto r = __builtin_amdgcn_permlane16_swap(__float_as_uint(v), __float_as_uint(v), false, false); return fmaxf(__uint_as_float(r[0]), __uint_as_float(r[1])); }
DI float max32(float v) { auto r = __builtin_amdgcn_permlane32_swap(__float_as_uint(v), __float_as_uint(v), false, false); return fmaxf(__uint_as_float(r[0]), __uint_as_float(r[1])); }
template <int CTRL> DI float dppf(float v) { return __int_as_float(__builtin_amdgcn_update_dpp(0, __float_as_int(v), CTRL, 0xf, 0xf, true)); }
DI float rowmax16(float v) {
  v = fmaxf(v, dppf<0xB1>(v)); v = fmaxf(v, dppf<0x4E>(v)); v = fmaxf(v, dppf<0x141>(v)); v = fmaxf(v, dppf<0x140>(v)); return v;
}

constexpr int EOFF = 143360;
constexpr int XOFF = EOFF + 17408;
constexpr int SYNC = XOFF + 2048;
constexpr int LDS_BYTES = SYNC + 256;

DI void signal_done(char* smem, unsigned* cnt, int lane) {
  wait_vm<0>();
  if (lane == 0) {
    const unsigned old = __hip_atomic_fetch_add((LDSP(unsigned))(smem + SYNC), 1u, __ATOMIC_RELAXED, __HIP_MEMORY_SCOPE_WORKGROUP);
    if ((old & 7u) == 7u) __hip_atomic_fetch_add((gu32*)cnt, 1u, __ATOMIC_RELAXED, __HIP_MEMORY_SCOPE_AGENT);
  }
}

DI void lds_wait(char* smem, int off, unsigned v) {
  volatile unsigned* w = (volatile unsigned*)(smem + SYNC + off);
  for (unsigned spins = 0; spins < 8000000u; ++spins) {
    if ((unsigned)__builtin_amdgcn_readfirstlane(*w) >= v) break;
    __builtin_amdgcn_s_sleep(1);
  }
  asm volatile("" ::: "memory");
}

enum { EPI_QKV = 0, EPI_FFN1 = 1, EPI_RES = 2 };

template <int BN> struct BL {
  static constexpr bool PADDED = (BN == 96);
  static constexpr int RB = BN * 2;
  static constexpr int GS = PADDED ? 784 : 4 * RB;
  static constexpr int STAGE = 16 * GS;
  static constexpr int NI = PADDED ? 16 : (64 * RB / 1024);
  static DI int sw(int row) { return BN == 64 ? (((row >> 1) & 1) | (((row >> 3) & 1) << 1)) : ((row & 3) | (((row >> 3) & 1) << 2)); }
};

template <int NK, int NS, int P0, int L, int NE> struct Sched {
  static constexpr int mn(int a, int b) { return a < b ? a : b; }
  static constexpr int up(int j) { return mn(mn(P0 + 2 * j, (j < 1 ? 1 : j) + NS - 1), NK); }
  static constexpr int behind(int j) { return (up(j) - 1 - j) * L + (up(j) == NK ? NE : 0); }
};

template <int BM, int BN, int KTOT, int N, int NA, int NB, int PB, int SUB, int PD, int EPI, bool STI, bool FIRST>
DI void gemm_phase(char* smem, const u16* Ap, const u16* Bp, const u16* aux, u16* out, float* stats, const int m0, const int n0,
                   unsigned* wcnt, unsigned wtarget, unsigned* scnt, unsigned ph, unsigned narr) {
  constexpr int WM = 4, WN = 2;
  constexpr int NK = KTOT / (64 * SUB);
  constexpr int ASUB = BM * 128, BSUB = BL<BN>::STAGE;
  constexpr int ASTG = ASUB * SUB, BSTG = BSUB * SUB;
  constexpr int BOFF = NA * ASTG;
  static_assert(BOFF + NB * BSTG <= EOFF, "rings exceed LDS");
  constexpr int MF = BM / WM / 16, NF = BN / WN / 16;
  constexpr int LA = BM / 16;
  constexpr int LB = BL<BN>::NI / 2;
  constexpr int LM = LA > LB ? LA : LB;
  constexpr bool LN = (EPI != EPI_RES);
  constexpr bool AHM = (EPI == EPI_RES && KTOT == 512);
  constexpr int NE = (EPI == EPI_RES) ? 2 : (STI ? 0 : (EPI == EPI_FFN1 ? 5 : 4));
  using SA = Sched<NK, NA, 2, LA * SUB, NE>;
  using SB = Sched<NK, NB, PB, LB * SUB, 0>;
  const unsigned lds0 = (unsigned)(size_t)(LDSP(char))smem;
  float* csb = (float*)(smem + XOFF);
  float* st = csb + 256;
  int tid = threadIdx.x;
  asm volatile("" : "+v"(tid));
  const int lane = tid & 63;
  const int wid = __builtin_amdgcn_readfirstlane(tid >> 6);
  const int l15 = lane & 15, g = lane >> 4;
  const int bq = l15 >> 2, bp = lane & 3;
  auto bfrag_off = [&](int f) -> unsigned {
    if (BL<BN>::PADDED) return (2 * g) * BL<BN>::GS + bq * BL<BN>::RB + f * 32 + bp * 8;
    const int row = 8 * g + bq;
    return row * BL<BN>::RB + ((f ^ BL<BN>::sw(row)) * 32) + bp * 8;
  };

  if (wid >= 8) {
    const int w4 = wid - 8;
    const bool isA = w4 < 2;
    const int w2 = w4 & 1;
    unsigned off[LM];
#pragma unroll
    for (int i = 0; i < LM; ++i) {
      if (isA) {
        const int ins = w2 * LA + i;
        const int row = ins * 8 + (lane >> 3), ch = (lane & 7) ^ ((row >> 1) & 7);
        off[i] = AHM ? (unsigned)((ch >> 1) * 65536 + row * 32 + (ch & 1) * 16) : (unsigned)(row * KTOT * 2 + ch * 16);
      } else {
        const int ins = w2 * LB + i;
        constexpr int CPR = BN / 8;
        if (BL<BN>::PADDED) {
          const int row = ins * 4 + lane / CPR, ch = lane % CPR;
          off[i] = (unsigned)(row * N * 2 + ch * 16);
        } else {
          const int row = ins * (64 / CPR) + lane / CPR;
          const int ch = (lane % CPR) ^ (BL<BN>::sw(row) << 1);
          off[i] = (unsigned)(row * N * 2 + ch * 16);
        }
      }
    }
    const char* abase = (const char*)Ap + (AHM ? (size_t)m0 * 32 : (size_t)m0 * KTOT * 2);
    const char* bbase = (const char*)Bp + (size_t)n0 * 2;
    auto extras = [&]() {
      if (EPI == EPI_RES) {
#pragma unroll
        for (int i = 0; i < 2; ++i) {
          const int row = (w4 * 2 + i) * 8 + (lane >> 3), ch = (lane & 7) ^ ((row >> 1) & 7);
          glds16s((const char*)aux + ((size_t)m0 * N + n0) * 2, (unsigned)(row * N * 2 + ch * 16), lds0 + EOFF + (w4 * 2 + i) * 1024);
        }
      } else if (!STI) {
#pragma unroll
        for (int i = 0; i < 4; ++i) glds16s((const char*)stats + (size_t)m0 * 128 + w4 * 256 + i * 4096, g * 1024 + l15 * 16, lds0 + EOFF + (w4 * 4 + i) * 1024);
        if (EPI == EPI_FFN1) glds16m<32>((const char*)aux + n0 * 2, (lane & 15) * 16, lds0 + EOFF + 16384);
      }
    };
    auto issueA = [&](int s) {
#pragma unroll
      for (int u = 0; u < SUB; ++u)
#pragma unroll
        for (int i = 0; i < LA; ++i)
          glds16s(abase + (size_t)(s * SUB + u) * (AHM ? 4 * 65536 : 128), off[i], lds0 + (s % NA) * ASTG + u * ASUB + (w2 * LA + i) * 1024);
      if (s == NK - 1) extras();
    };
    auto issueB = [&](int s) {
#pragma unroll
      for (int u = 0; u < SUB; ++u) {
        const unsigned slot = lds0 + BOFF + (s % NB) * BSTG + u * BSUB;
        const char* src = bbase + (size_t)(s * SUB + u) * 64 * N * 2;
#pragma unroll
        for (int i = 0; i < LB; ++i) {
          if (BL<BN>::PADDED) glds16m<BL<BN>::PADDED ? 4 * (BN / 8) : 64>(src, off[i], slot + (w2 * LB + i) * BL<BN>::GS);
          else glds16(src, off[i], slot + (w2 * LB + i) * 1024);
        }
      }
    };
    if (isA) {
      if (!FIRST) lds_wait(smem, 8, ph);
#pragma unroll
      for (int s = 0; s < SA::up(0); ++s) issueA(s);
    } else {
      if (!FIRST && !EARLYB) lds_wait(smem, 0, narr);
#pragma unroll
      for (int s = 0; s < SB::up(0); ++s) issueB(s);
    }
    const bf16x8 ONES = __builtin_bit_cast(bf16x8, u32x4{0x3f803f80u, 0x3f803f80u, 0x3f803f80u, 0x3f803f80u});
    const unsigned c_rd0 = lds0 + BOFF + bfrag_off(w4), c_rd1 = lds0 + BOFF + bfrag_off(w4 + 4);
    const unsigned a_rd = lds0 + (w4 * 32 + l15) * 128 + ((g ^ (l15 >> 1)) * 16);
    f32x4 cs0 = {0.f, 0.f, 0.f, 0.f}, cs1 = cs0, rs[2] = {cs0, cs0}, rq[2] = {cs0, cs0};
#pragma unroll
    for (int j = 0; j < NK; ++j) {
      if (isA) {
        wait_bar_n(SA::behind(j));
#pragma unroll
        for (int s = SA::up(j); s < SA::up(j + 1); ++s) issueA(s);
      } else {
        wait_bar_n(SB::behind(j) + ((j > NK - 3 + 0) && (NK - 2 >= 1) ? ((j >= NK - 1) ? NE : 0) : 0));
#pragma unroll
        for (int s = SB::up(j); s < SB::up(j + 1); ++s) issueB(s);
        if (j == NK - 2) extras();
      }
      if (LN) {
#pragma unroll
        for (int ks = 0; ks < 2; ++ks) {
          const unsigned o = (j % NB) * BSTG + ks * 8 * BL<BN>::GS;
          cs0 = __builtin_amdgcn_mfma_f32_16x16x32_bf16(CAT8(trread(c_rd0 + o), trread(c_rd0 + o + BL<BN>::GS)), ONES, cs0, 0, 0, 0);
          cs1 = __builtin_amdgcn_mfma_f32_16x16x32_bf16(CAT8(trread(c_rd1 + o), trread(c_rd1 + o + BL<BN>::GS)), ONES, cs1, 0, 0, 0);
          if (STI) {
#pragma unroll
            for (int mf = 0; mf < 2; ++mf) {
              const bf16x8 a = ldsread16((a_rd ^ (ks * 64)) + (j % NA) * ASTG + mf * 2048);
              rs[mf] = __builtin_amdgcn_mfma_f32_16x16x32_bf16(ONES, a, rs[mf], 0, 0, 0);
              rq[mf] = __builtin_amdgcn_mfma_f32_16x16x32_bf16(a, a, rq[mf], 0, 0, 0);
            }
          }
        }
        PIN(cs0); PIN(cs1);
        if (STI) { PIN(rs[0]); PIN(rs[1]); PIN(rq[0]); PIN(rq[1]); }
      }
    }
    if (LN) {
      if (l15 == 0) {
        *(f32x4*)(csb + w4 * 16 + g * 4) = cs0;
        *(f32x4*)(csb + 64 + w4 * 16 + g * 4) = cs1;
      }
      if (STI) {
#pragma unroll
        for (int mf = 0; mf < 2; ++mf) {
          const int jj = l15 & 3;
          const float d = (jj == 0) ? rq[mf][0] : (jj == 1) ? rq[mf][1] : (jj == 2) ? rq[mf][2] : rq[mf][3];
          if (g == (l15 >> 2)) *(f32x2*)(st + (w4 * 32 + mf * 16 + l15) * 2) = f32x2{rs[mf][0], d};
        }
      } else {
        wait_vm<0>();
        f32x4 v = *(const f32x4*)(smem + EOFF + (w4 * 4) * 1024 + lane * 16);
#pragma unroll
        for (int i = 1; i < 4; ++i) v += *(const f32x4*)(smem + EOFF + (w4 * 4 + i) * 1024 + lane * 16);
#pragma unroll
        for (int i = 0; i < 4; ++i) v[i] = sum32(sum16(v[i]));
        if (g == 0) *(f32x4*)(st + (w4 * 32 + 2 * l15) * 2) = v;
      }
    }
    wait_bar<0>();
    return;
  }

  const int wm = wid % WM, wn = wid / WM;
  const int trow = m0 + wm * (MF * 16) + l15;
  const int ncol = n0 + wn * NF * 16;
  const unsigned a_rd = lds0 + (wm * (MF * 16) + l15) * 128 + ((g ^ (l15 >> 1)) * 16);
  unsigned b_rd[BL<BN>::PADDED ? 1 : NF];
#pragma unroll
  for (int nf = 0; nf < (BL<BN>::PADDED ? 1 : NF); ++nf) b_rd[nf] = lds0 + BOFF + bfrag_off(wn * NF + nf);
  f32x4 acc[MF][NF];
#pragma unroll
  for (int a = 0; a < MF; ++a)
#pragma unroll
    for (int b = 0; b < NF; ++b) acc[a][b] = f32x4{0.f, 0.f, 0.f, 0.f};

  constexpr int KS = 2 * SUB, GPS = KS / PD, NG = NK * GPS;
  bf16x8 af[2][PD][MF], bfr[2][PD][NF];
  auto rdfrags = [&](int gi) {
    const int pb = gi & 1, s = gi / GPS;
#pragma unroll
    for (int q = 0; q < PD; ++q) {
      const int k = (gi % GPS) * PD + q;
#pragma unroll
      for (int mf = 0; mf < MF; ++mf) af[pb][q][mf] = ldsread16((a_rd ^ ((k & 1) * 64)) + (s % NA) * ASTG + (k >> 1) * ASUB + mf * 2048);
#pragma unroll
      for (int nf = 0; nf < NF; ++nf) {
        const unsigned o = (BL<BN>::PADDED ? b_rd[0] + nf * 32 : b_rd[nf]) + (s % NB) * BSTG + (k >> 1) * BSUB + (k & 1) * 8 * BL<BN>::GS;
        bfr[pb][q][nf] = CAT8(trread(o), trread(o + BL<BN>::GS));
      }
    }
  };
  if (!FIRST && wid == 0) { wait_cnt(wcnt, wtarget, lane); *(volatile unsigned*)(smem + SYNC + 8) = ph; }
  lds_bar();
  rdfrags(0);
#pragma unroll
  for (int gi = 0; gi < NG; ++gi) {
    if (gi + 1 < NG) {
      if ((gi + 1) % GPS == 0) lds_bar();
      SBAR();
      rdfrags(gi + 1);
    }
    const int pb = gi & 1;
#pragma unroll
    for (int q = 0; q < PD; ++q)
#pragma unroll
      for (int nf = 0; nf < NF; ++nf)
#pragma unroll
        for (int mf = 0; mf < MF; ++mf)
          acc[mf][nf] = __builtin_amdgcn_mfma_f32_16x16x32_bf16(bfr[pb][q][nf], af[pb][q][mf], acc[mf][nf], 0, 0, 0);
    if (gi + 1 < NG) {
      constexpr int NM = PD * MF * NF, ND = PD * (MF + 2 * NF);
      constexpr int PER = (ND + NM - 1) / NM;
#pragma unroll
      for (int i = 0; i < NM; ++i) {
        __builtin_amdgcn_sched_group_barrier(0x100, PER, 0);
        __builtin_amdgcn_sched_group_barrier(0x8, 1, 0);
      }
    }
#pragma unroll
    for (int mf = 0; mf < MF; ++mf)
#pragma unroll
      for (int nf = 0; nf < NF; ++nf) PIN(acc[mf][nf]);
  }
  lds_bar();
  if (LN) {
    f32x4 c0[NF];
    float rstd[MF], nmr[MF];
#pragma unroll
    for (int nf = 0; nf < NF; ++nf) c0[nf] = *(const f32x4*)(csb + (wn * NF + nf) * 16 + g * 4);
#pragma unroll
    for (int mf = 0; mf < MF; ++mf) {
      const f32x2 a = *(const f32x2*)(st + (wm * (MF * 16) + mf * 16 + l15) * 2);
      const float mean = a[0] * (1.f / KTOT);
      const float var = fmaxf(a[1] * (1.f / KTOT) - mean * mean, 0.f);
      rstd[mf] = __builtin_amdgcn_rsqf(var + 1e-5f);
      nmr[mf] = -mean * rstd[mf];
    }
    if (EPI == EPI_QKV) {
      const int tk = trow + (g & 1) * 16;
      const unsigned lin = tk * 16 + (g >> 1) * 8;
      const unsigned kof = (tk >> 5) * 512 + (g >> 1) * 256 + (tk & 31) * 8;
#pragma unroll
      for (int nf = 0; nf < NF; ++nf) {
        const int hh = (ncol >> 4) + nf;
        const bool isq = hh < 32, isk = (hh >> 5) == 1;
        const float sc = isq ? 0.25f * 1.4426950408889634f : 1.f;
        u16* base = out + (size_t)hh * (SEQ * 16);
        u32x2 o[MF];
#pragma unroll
        for (int mf = 0; mf < MF; ++mf) {
          const f32x4 y = acc[mf][nf] * (rstd[mf] * sc) + c0[nf] * (nmr[mf] * sc);
          o[mf] = u32x2{cvtpk(y[0], y[1]), cvtpk(y[2], y[3])};
        }
        st16(base + (isk ? kof : lin), pair16(o[0], o[MF - 1]));
      }
    } else {
      u32x2 o[MF][NF];
#pragma unroll
      for (int nf = 0; nf < NF; ++nf) {
        const u32x2 bb = *(const u32x2*)(smem + EOFF + 16384 + ((wn * NF + nf) * 16 + g * 4) * 2);
        const f32x4 bias = {bflo(bb[0]), bfhi(bb[0]), bflo(bb[1]), bfhi(bb[1])};
#pragma unroll
        for (int mf = 0; mf < MF; ++mf) {
          const f32x4 h = acc[mf][nf] * rstd[mf] + (c0[nf] * nmr[mf] + bias);
          const f32x4 t = h * 1.4426950408889634f;
          f32x4 u, rc;
#pragma unroll
          for (int j = 0; j < 4; ++j) u[j] = __builtin_amdgcn_exp2f(t[j]);
          const f32x4 d = u * (u * -0.5f - 1.f) - 1.f;
#pragma unroll
          for (int j = 0; j < 4; ++j) rc[j] = __builtin_amdgcn_rcpf(d[j]);
          const f32x4 a = h * rc + h;
          o[mf][nf] = u32x2{cvtpk(a[0], a[1]), cvtpk(a[2], a[3])};
          if (nf & 1) st16pair(out + (size_t)(trow + mf * 16) * N + ncol + (nf - 1) * 16, g, o[mf][nf - 1], o[mf][nf]);
        }
      }
    }
  } else {
#pragma unroll
    for (int mf = 0; mf < MF; ++mf) {
      const int tok = trow + mf * 16;
      float s1 = 0.f, s2 = 0.f;
      u32x2 o[NF];
#pragma unroll
      for (int nf = 0; nf < NF; ++nf) {
        const u32x2 rr = *(const u32x2*)(smem + EOFF + (wm * (MF * 16) + mf * 16 + l15) * 128 + ((((wn * NF + nf) * 2 + (g >> 1)) ^ (l15 >> 1)) * 16) + (g & 1) * 8);
        const f32x4 r = acc[mf][nf];
        o[nf] = u32x2{cvtpk(r[0] + bflo(rr[0]), r[1] + bfhi(rr[0])), cvtpk(r[2] + bflo(rr[1]), r[3] + bfhi(rr[1]))};
        const float e0 = bflo(o[nf][0]), e1 = bfhi(o[nf][0]), e2 = bflo(o[nf][1]), e3 = bfhi(o[nf][1]);
        s1 += (e0 + e1) + (e2 + e3);
        s2 += (e0 * e0 + e1 * e1) + (e2 * e2 + e3 * e3);
        if (nf & 1) st16pair(out + (size_t)tok * N + ncol + (nf - 1) * 16, g, o[nf - 1], o[nf]);
      }
      s1 = sum32(sum16(s1)); s2 = sum32(sum16(s2));
      if (g == 0) st8(stats + (tok >> 7) * 4096 + (ncol >> 5) * 256 + (tok & 127) * 2, f32x2{s1, s2});
    }
  }
  if (!FIRST) signal_done(smem, scnt, lane);
}

constexpr int AT_K = 0, AT_V = 65536, AT_ONE = 131072, AT_Q = AT_ONE + 6144, AT_PF = AT_Q + 8 * 9 * 256;
static_assert(AT_PF + 1024 <= XOFF, "attention LDS");

#define ATT_HALF(SC, OO, VC, MID)                                                                            \
  {                                                                                                          \
    _Pragma("unroll") for (int i = 0; i < 4; ++i) SC[i] = __builtin_amdgcn_exp2f(SC[i]);                     \
    SBAR(); MID; SBAR();                                                                                     \
    _Pragma("unroll") for (int i = 4; i < 9; ++i) SC[i] = __builtin_amdgcn_exp2f(SC[i]);                     \
    u32x4 p0 = {cvtpk(SC[0], SC[1]), cvtpk(SC[2], SC[3]), cvtpk(SC[4], SC[5]), cvtpk(SC[6], SC[7])};         \
    PIN(p0); SBAR();                                                                                         \
    SC[9] = __builtin_amdgcn_exp2f(SC[9]); SBAR();                                                           \
    OO = MFMA32(CAT8(VC[0], VC[1]), __builtin_bit_cast(bf16x8, p0), OO); SBAR();                             \
    _Pragma("unroll") for (int i = 10; i < 16; ++i) SC[i] = __builtin_amdgcn_exp2f(SC[i]);                   \
    u32x4 p1 = {cvtpk(SC[8], SC[9]), cvtpk(SC[10], SC[11]), cvtpk(SC[12], SC[13]), cvtpk(SC[14], SC[15])};   \
    OO = MFMA32(CAT8(VC[2], VC[3]), __builtin_bit_cast(bf16x8, p1), OO); SBAR();                             \
  }
#define ATT_TILE(VC, VN, T)                                                                                  \
  ATT_HALF(s0, o0, VC, { s1 = MFMA32(kf, qf1, cm1); PIN(s1); kf = ldsread16(kaddr + ((T) + 1) * 1024);       \
       VN[0] = trread(vaddr + ((T) + 1) * 1024); VN[1] = trread(vaddr + ((T) + 1) * 1024 + 256); })          \
  ATT_HALF(s1, o1, VC, { s0 = MFMA32(kf, qf0, cm0); PIN(s0);                                                 \
       VN[2] = trread(vaddr + ((T) + 1) * 1024 + 512); VN[3] = trread(vaddr + ((T) + 1) * 1024 + 768); })

template <bool FAST>
DI void attn_pass(unsigned lds0, int lane, int ks, const bf16x8 qf0, const bf16x8 qf1, float nm0, float nm1, f32x16& o0, f32x16& o1) {
  const int h = lane >> 5;
  unsigned kaddr = lds0 + AT_K + ks * 32768 + h * 512 + (lane & 31) * 16;
  const bool cl = (lane >> 4) & 1;
  const int qq = (lane & 15) >> 2, pp = lane & 3;
  unsigned vaddr = cl ? lds0 + AT_ONE + (4 * (h ^ 1) + qq) * 32 + pp * 8 : lds0 + AT_V + ks * 32768 + (4 * h + qq) * 32 + pp * 8;
  const unsigned vstep = cl ? 0u : 4096u;
  f32x16 cm0, cm1, s0, s1;
  s16x4 vA[4], vB[4];
#pragma unroll
  for (int i = 0; i < 16; ++i) { cm0[i] = FAST ? 0.f : nm0; cm1[i] = FAST ? 0.f : nm1; o0[i] = 0.f; o1[i] = 0.f; }
  bf16x8 kf = ldsread16(kaddr);
  s0 = MFMA32(kf, qf0, cm0);
  vA[0] = trread(vaddr); vA[1] = trread(vaddr + 256); vA[2] = trread(vaddr + 512); vA[3] = trread(vaddr + 768);
  for (int it = 0; it < 8; ++it) {
    PIN(kaddr); PIN(vaddr);
    ATT_TILE(vA, vB, 0) ATT_TILE(vB, vA, 1) ATT_TILE(vA, vB, 2) ATT_TILE(vB, vA, 3)
    kaddr += 4096; vaddr += vstep;
    lds_bar();
  }
}

DI void attn_phase(char* smem, const u16* QKV, u16* O, const u16* wo, const u16* w1, const u16* w2, unsigned* cq, bool wait, unsigned* scnt) {
  const unsigned lds0 = (unsigned)(size_t)(LDSP(char))smem;
  int tid = threadIdx.x;
  asm volatile("" : "+v"(tid));
  const int lane = tid & 63;
  const int wid = __builtin_amdgcn_readfirstlane(tid >> 6);
  const int bid = blockIdx.x;
  const int head = (bid & 7) * 4 + ((bid >> 3) & 3), qb = bid >> 5;
  volatile unsigned* flag = (volatile unsigned*)(smem + SYNC + 4);
  if (wid >= 8) {
    const int w4 = wid - 8;
    const unsigned l16 = lane * 16;
#pragma unroll
    for (int i = 0; i < 2; ++i) { const int t = (w4 * 2 + i) * 64 + lane; if (t < 384) *(u32x4*)(smem + AT_ONE + t * 16) = u32x4{(t & 1) ? 0u : 0x3f80u, 0u, 0u, 0u}; }
    if (tid == 767) *flag = 0u;
    if (wait) {
      wait_cnt(cq + (head / 6) * 32, 16, lane); wait_cnt(cq + ((32 + 32 * (w4 >> 1) + head) / 6) * 32, 16, lane);
    }
    const char* qsrc = (const char*)QKV + ((size_t)head * SEQ + qb * 256) * 32 + w4 * 2048;
    const char* src = (const char*)QKV + (size_t)(32 + 32 * (w4 >> 1) + head) * 65536 + (w4 & 1) * 32768;
    const unsigned dst = lds0 + (w4 >> 1) * 65536 + (w4 & 1) * 32768;
    glds16s(qsrc, l16, lds0 + AT_Q + w4 * 2048); glds16s(qsrc + 1024, l16, lds0 + AT_Q + w4 * 2048 + 1024);
#pragma unroll
    for (int i = 0; i < 8; ++i) glds16s(src + i * 1024, l16, dst + i * 1024);
    wait_bar<0>();
#pragma unroll
    for (int i = 8; i < 32; ++i) glds16s(src + i * 1024, l16, dst + i * 1024);
    wait_bar<0>();
    {
      const int u = (bid >> 3) * 4 + w4;
      const char* p1 = (u < 28) ? (const char*)wo + u * 18432 : (const char*)w1 + (u - 28) * 18432;
      const int n1 = (u < 28) ? (u == 27 ? 26624 : 18432) : (u == 127 ? 10240 : 18432);
      const char* p2 = (const char*)w2 + (size_t)(bid * 4 + w4) * 1792;
      int off = 0;
      for (int it = 1; it < 8; ++it) {
#pragma unroll
        for (int i = 0; i < 4; ++i) { if (off < n1) glds16(p1 + off, l16, lds0 + AT_PF); off += 1024; }
        if (it == 1) { glds16(p2, l16, lds0 + AT_PF); glds16m<48>(p2 + 1024, l16, lds0 + AT_PF); }
        if (it == 7) wait_bar<0>(); else lds_bar();
      }
    }
    lds_bar();
    if (__builtin_amdgcn_readfirstlane(*flag) != 0u) {
      for (int it = 0; it < 9; ++it) lds_bar();
    }
    return;
  }
  const int wq = wid & 3, ks = wid >> 2;
  lds_bar();
  const bf16x8 qf0 = ldsread16(lds0 + AT_Q + wq * 2048 + (lane & 31) * 32 + (lane >> 5) * 16);
  const bf16x8 qf1 = ldsread16(lds0 + AT_Q + wq * 2048 + 1024 + (lane & 31) * 32 + (lane >> 5) * 16);
  f32x16 o0, o1;
  attn_pass<true>(lds0, lane, ks, qf0, qf1, 0.f, 0.f, o0, o1);
  float* xb = (float*)(smem + AT_Q);
  if (lane < 32 && !(o0[8] >= 8.67e-19f && o0[8] <= 1.26e30f && o1[8] >= 8.67e-19f && o1[8] <= 1.26e30f)) *flag = 1u;
#pragma unroll
  for (int i = 0; i < 9; ++i) xb[(wid * 9 + i) * 64 + lane] = ks ? o0[i] : o1[i];
  lds_bar();
  if (__builtin_amdgcn_readfirstlane(*flag) != 0u) {
    float m0 = -3.0e38f, m1 = -3.0e38f;
    const unsigned kaddr = lds0 + AT_K + (lane >> 5) * 512 + (lane & 31) * 16;
    f32x16 z;
#pragma unroll
    for (int i = 0; i < 16; ++i) z[i] = 0.f;
    for (int t = 0; t < 64; ++t) {
      const bf16x8 kf = ldsread16(kaddr + t * 1024);
      const f32x16 sa = MFMA32(kf, qf0, z), sb = MFMA32(kf, qf1, z);
#pragma unroll
      for (int i = 0; i < 16; ++i) { m0 = fmaxf(m0, sa[i]); m1 = fmaxf(m1, sb[i]); }
    }
    m0 = max32(m0); m1 = max32(m1);
    attn_pass<false>(lds0, lane, ks, qf0, qf1, -m0, -m1, o0, o1);
#pragma unroll
    for (int i = 0; i < 9; ++i) xb[(wid * 9 + i) * 64 + lane] = ks ? o0[i] : o1[i];
    lds_bar();
  }
  f32x16 o = ks ? o1 : o0;
#pragma unroll
  for (int i = 0; i < 9; ++i) o[i] += xb[((wid ^ 4) * 9 + i) * 64 + lane];
  auto lr = __builtin_amdgcn_permlane32_swap(__float_as_uint(o[8]), __float_as_uint(o[8]), false, false);
  const float rl = __builtin_amdgcn_rcpf(__uint_as_float(lr[0]));
  unsigned x0 = cvtpk(o[0] * rl, o[1] * rl), x1 = cvtpk(o[2] * rl, o[3] * rl);
  unsigned y0 = cvtpk(o[4] * rl, o[5] * rl), y1 = cvtpk(o[6] * rl, o[7] * rl);
  auto r0 = __builtin_amdgcn_permlane32_swap(x0, y0, false, false);
  auto r1 = __builtin_amdgcn_permlane32_swap(x1, y1, false, false);
  const int q0 = qb * 256 + wq * 64 + ks * 32;
  st16(O + ((size_t)head * SEQ + q0 + (lane & 31)) * 16 + (lane >> 5) * 8, u32x4{r0[0], r1[0], r0[1], r1[1]});
  signal_done(smem, scnt, lane);
}

constexpr size_t MB = 1 << 20;
constexpr size_t WS_QKV = 0, WS_O = 12 * MB, WS_X1 = 16 * MB, WS_X2 = 20 * MB, WS_H = 22 * MB, WS_ST = 38 * MB, WS_CNT = 39 * MB;
constexpr int C_A = 0, C_1 = 8, C_2 = 24, C_3 = 40, C_LAYER = 56, C_Q = 112, C_TOTAL = 128;

struct Params {
  const u16 *x, *wqkv, *wo, *w1, *b1, *w2;
  u16* out;
  char* ws;
};

extern "C" __global__ __launch_bounds__(768, 3) void k_mega(Params p) {
  __shared__ __attribute__((aligned(256))) char smem[LDS_BYTES];
  if (threadIdx.x == 0) { *(unsigned*)(smem + SYNC) = 0u; *(unsigned*)(smem + SYNC + 8) = 0u; }
  if (__builtin_amdgcn_readfirstlane(threadIdx.x) >= 512) __builtin_amdgcn_s_setprio(3);
  const int bid = blockIdx.x, idx = bid >> 3;
  const int team = (bid & 7) * 2 + (idx >> 4), mem = idx & 15;
  unsigned* cnt = (unsigned*)(p.ws + WS_CNT);
  unsigned narr = 0;
  for (int layer = 0; layer < 2; ++layer) {
    unsigned* cl = cnt + layer * C_LAYER * 32;
    u16* O = (u16*)(p.ws + WS_O + layer * 2 * MB);
    u16* X1 = (u16*)(p.ws + WS_X1 + layer * 2 * MB);
    u16* X2 = (u16*)(p.ws + WS_X2);
    u16* H = (u16*)(p.ws + WS_H + layer * 8 * MB);
    float* st1 = (float*)(p.ws + WS_ST + layer * 2 * 262144);
    float* st2 = st1 + 65536;
    attn_phase(smem, (const u16*)(p.ws + WS_QKV + layer * 6 * MB), O, p.wo, p.w1, p.w2, cnt + C_Q * 32, layer != 0, cl + (C_A + (bid >> 5)) * 32);
    narr += 8;
    gemm_phase<64, 64, 512, 512, 4, 4, PB_O, 2, 2, EPI_RES, true, false>(smem, O, p.wo, layer ? X2 : p.x, X1, st1, team * 128 + (mem >> 3) * 64, (mem & 7) * 64,
                                                                    cl + (C_A + (team >> 1)) * 32, 32, cl + (C_1 + team) * 32, 1 + layer * 8, narr);
    narr += 8;
    if (mem < 14) {
      gemm_phase<128, 128, 512, 1792, 3, 5, PB_1, 1, 2, EPI_FFN1, false, false>(smem, X1, p.w1, p.b1, H, st1, team * 128, mem * 128,
                                                                          cl + (C_1 + team) * 32, 16, cl + (C_2 + team) * 32, 2 + layer * 8, narr);
      narr += 8;
    }
    gemm_phase<64, 64, 1792, 512, 3, 5, PB_2, 2, 2, EPI_RES, true, false>(smem, H, p.w2, X1, layer ? p.out : X2, st2, team * 128 + (mem >> 3) * 64, (mem & 7) * 64,
                                                                     cl + (C_2 + team) * 32, 14, cl + (C_3 + team) * 32, 3 + layer * 8, narr);
    narr += 8;
    if (layer == 0) {
      gemm_phase<128, 96, 512, 1536, 3, 7, PB_Q, 1, 2, EPI_QKV, false, false>(smem, X2, p.wqkv, nullptr, (u16*)(p.ws + WS_QKV + 6 * MB), st2, team * 128, mem * 96,
                                                                        cl + (C_3 + team) * 32, 16, cnt + (C_Q + mem) * 32, 4, narr);
      narr += 8;
    }
  }
}
extern "C" __global__ void k_zend() {}

extern "C" __global__ __launch_bounds__(768, 3) void k_qkv1(Params p) {
  __shared__ __attribute__((aligned(256))) char smem[LDS_BYTES];
  const int bid = blockIdx.x, xcd = bid & 7, idx = bid >> 3;
  if (__builtin_amdgcn_readfirstlane(threadIdx.x) >= 512) __builtin_amdgcn_s_setprio(3);
  if (bid == 0 && threadIdx.x < C_TOTAL) *(unsigned*)(p.ws + WS_CNT + threadIdx.x * 128) = 0u;
  unsigned codev = 0;
  {
    unsigned long long lo, hi;
    asm volatile("s_getpc_b64 s[20:21]\n\ts_add_u32 s20, s20, k_mega@rel32@lo+4\n\ts_addc_u32 s21, s21, k_mega@rel32@hi+12\n\ts_mov_b64 %0, s[20:21]\n\t"
                 "s_getpc_b64 s[20:21]\n\ts_add_u32 s20, s20, k_zend@rel32@lo+4\n\ts_addc_u32 s21, s21, k_zend@rel32@hi+12\n\ts_mov_b64 %1, s[20:21]"
                 : "=s"(lo), "=s"(hi)::"s20", "s21", "scc");
    const unsigned long long a = lo + (unsigned long long)(idx * 2048 + threadIdx.x * 4);
    if (threadIdx.x < 512 && a + 4 <= hi) codev = *(const __attribute__((address_space(1))) unsigned*)a;
  }
  gemm_phase<128, 96, 512, 1536, 3, 3, 2, 1, 2, EPI_QKV, true, true>(smem, p.x, p.wqkv, nullptr, (u16*)(p.ws + WS_QKV), nullptr,
                                                                   ((xcd >> 1) * 4 + idx / 8) * 128, ((xcd & 1) * 8 + idx % 8) * 96, nullptr, 0, nullptr, 0, 0);
  asm volatile("" ::"v"(codev));
}

extern "C" void kernel_launch(void* const* d_in, const int* in_sizes, int n_in, void* d_out, int out_size, void* d_ws,
                              size_t ws_size, hipStream_t stream) {
  Params p;
  p.x = (const u16*)d_in[0]; p.wqkv = (const u16*)d_in[1]; p.wo = (const u16*)d_in[2];
  p.w1 = (const u16*)d_in[4]; p.b1 = (const u16*)d_in[5]; p.w2 = (const u16*)d_in[6];
  p.out = (u16*)d_out; p.ws = (char*)d_ws;
  k_qkv1<<<256, 768, 0, stream>>>(p);
  k_mega<<<256, 768, 0, stream>>>(p);
}
```

```cpp
#include <hip/hip_runtime.h>
#include <hip/hip_bf16.h>
#include <stdint.h>

typedef short bf16x8 __attribute__((ext_vector_type(8)));
typedef short s16x4 __attribute__((ext_vector_type(4)));
typedef __bf16 bf16x2 __attribute__((ext_vector_type(2)));
typedef float f32x4 __attribute__((ext_vector_type(4)));
typedef float f32x16 __attribute__((ext_vector_type(16)));
typedef unsigned u32x2 __attribute__((ext_vector_type(2)));
typedef unsigned u32x4 __attribute__((ext_vector_type(4)));
typedef unsigned short u16;
#define LDSP(T) __attribute__((address_space(3))) T*
#define DI __device__ __forceinline__

#define NOCP 0
#define POLLSLEEP 0
#define EARLYB 0
#define PB_O 3
#define PB_1 3
#define PB_2 3
#define PB_Q 4
#define SC1LD 1
#define ROT 0
#define CFG_O 4, 4, 3, 2, 2
#define CFG_1 3, 5, 3, 1, 2
#define CFG_2 3, 5, 3, 2, 2
#define CFG_Q 3, 7, 4, 1, 2
#define CFG_F 3, 3, 2, 1, 2
#define KSPLIT 0
#define TOUCH 0
#define NPOLL 1
#define FLAGS 0
#define LDPOLL 0
#define ABL 0
constexpr int SEQ = 2048, DM = 512, NH = 32, DH = 16, FF = 1792;

typedef float f32x2 __attribute__((ext_vector_type(2)));
DI unsigned cvtpk(float lo, float hi) { return __builtin_bit_cast(unsigned, __builtin_convertvector(f32x2{lo, hi}, bf16x2)); }
DI float bflo(unsigned u) { return __uint_as_float(u << 16); }
DI float bfhi(unsigned u) { return __uint_as_float(u & 0xffff0000u); }
DI float dot2(unsigned a, unsigned b, float c) {
  return __builtin_amdgcn_fdot2_f32_bf16(__builtin_bit_cast(bf16x2, a), __builtin_bit_cast(bf16x2, b), c, false);
}
DI void glds16(const void* sbase, unsigned voff, unsigned lds_addr) {
  unsigned sv;
  asm volatile("s_mov_b32 %0, m0\n\ts_mov_b32 m0, %3\n\ts_nop 0\n\tglobal_load_lds_dwordx4 %1, %2\n\ts_mov_b32 m0, %0"
               : "=&s"(sv) : "v"(voff), "s"(sbase), "s"(lds_addr) : "memory");
}
DI void glds16s(const void* sbase, unsigned voff, unsigned lds_addr) {
  if (!SC1LD) { glds16(sbase, voff, lds_addr); return; }
  unsigned sv;
  asm volatile("s_mov_b32 %0, m0\n\ts_mov_b32 m0, %3\n\ts_nop 0\n\tglobal_load_lds_dwordx4 %1, %2 sc1\n\ts_mov_b32 m0, %0"
               : "=&s"(sv) : "v"(voff), "s"(sbase), "s"(lds_addr) : "memory");
}
template <int NL> DI void glds16m(const void* sbase, unsigned voff, unsigned lds_addr) {
  unsigned sv;
  asm volatile("s_mov_b32 %0, m0\n\ts_mov_b32 m0, %3\n\ts_mov_b32 exec_hi, %4\n\tglobal_load_lds_dwordx4 %1, %2\n\ts_mov_b32 exec_hi, -1\n\ts_mov_b32 m0, %0"
               : "=&s"(sv) : "v"(voff), "s"(sbase), "s"(lds_addr), "i"((unsigned)((1ull << (NL - 32)) - 1)) : "memory");
}
#define SBAR() __builtin_amdgcn_sched_barrier(0)
#define PIN(x) asm volatile("" : "+v"(x))
#define MFMA32(a, b, c) __builtin_amdgcn_mfma_f32_32x32x16_bf16(a, b, c, 0, 0, 0)
#define CAT8(a, b) __builtin_shufflevector(a, b, 0, 1, 2, 3, 4, 5, 6, 7)
template <int N> DI void wait_bar() { asm volatile("s_waitcnt vmcnt(%0) lgkmcnt(0)\n\ts_barrier" ::"i"(N) : "memory"); }
DI void wait_bar_n(int n) { asm volatile("s_waitcnt vmcnt(%0) lgkmcnt(0)\n\ts_barrier" ::"i"(n) : "memory"); }
template <int N> DI void wait_vm() { asm volatile("s_waitcnt vmcnt(%0)" ::"i"(N) : "memory"); }
DI void lds_bar() { asm volatile("s_waitcnt lgkmcnt(0)\n\ts_barrier" ::: "memory"); }
#define STF 1
DI void st16(void* ptr, u32x4 v) {
  if (ABL & 32) { PIN(v); return; }
  if (STF == 0) *(u32x4*)ptr = v;
  else asm volatile("global_store_dwordx4 %0, %1, off sc1\n\ts_nop 1" ::"v"(ptr), "v"(v) : "memory");
}
DI u32x4 pair16(u32x2 a, u32x2 b) {
  auto r0 = __builtin_amdgcn_permlane16_swap(a[0], b[0], false, false);
  auto r1 = __builtin_amdgcn_permlane16_swap(a[1], b[1], false, false);
  return u32x4{r0[0], r1[0], r0[1], r1[1]};
}
DI void st16pair(u16* rowp, int g, u32x2 a, u32x2 b) { st16(rowp + (g & 1) * 16 + (g >> 1) * 8, pair16(a, b)); }
DI void st8(void* ptr, f32x2 v) { asm volatile("global_store_dwordx2 %0, %1, off sc1" ::"v"(ptr), "v"(v) : "memory"); }
typedef __attribute__((address_space(1))) unsigned gu32;
DI void wait_cnt(unsigned* c, unsigned target, int lane) {
  for (unsigned spins = 0; spins < 200000u; ++spins) {
    if (FLAGS) {
      unsigned v = 1;
      if (lane < (int)target) v = __hip_atomic_load((gu32*)(c + lane), __ATOMIC_RELAXED, __HIP_MEMORY_SCOPE_AGENT);
      if (__all(v != 0u)) break;
    } else {
      unsigned v = 0;
      if (lane == 0) v = __hip_atomic_load((gu32*)c, __ATOMIC_RELAXED, __HIP_MEMORY_SCOPE_AGENT);
      if ((unsigned)__builtin_amdgcn_readfirstlane(v) >= target) break;
    }
    if (POLLSLEEP) __builtin_amdgcn_s_sleep(POLLSLEEP);
  }
  asm volatile("" ::: "memory");
}
DI void acquire_cu() {
  if (SC1LD) return;
  __builtin_amdgcn_fence(__ATOMIC_ACQUIRE, "agent");
  asm volatile("s_waitcnt vmcnt(0)" ::: "memory");
}
DI s16x4 trread(unsigned addr) {
  return __builtin_bit_cast(s16x4, __builtin_amdgcn_ds_read_tr16_b64_v4i16((LDSP(s16x4))(size_t)addr));
}
DI bf16x8 ldsread16(unsigned addr) { return *(const LDSP(bf16x8))(size_t)addr; }
DI float sum16(float v) { auto r = __builtin_amdgcn_permlane16_swap(__float_as_uint(v), __float_as_uint(v), false, false); return __uint_as_float(r[0]) + __uint_as_float(r[1]); }
DI float sum32(float v) { auto r = __builtin_amdgcn_permlane32_swap(__float_as_uint(v), __float_as_uint(v), false, false); return __uint_as_float(r[0]) + __uint_as_float(r[1]); }
DI float max16(float v) { auto r = __builtin_amdgcn_permlane16_swap(__float_as_uint(v), __float_as_uint(v), false, false); return fmaxf(__uint_as_float(r[0]), __uint_as_float(r[1])); }
DI float max32(float v) { auto r = __builtin_amdgcn_permlane32_swap(__float_as_uint(v), __float_as_uint(v), false, false); return fmaxf(__uint_as_float(r[0]), __uint_as_float(r[1])); }
template <int CTRL> DI float dppf(float v) { return __int_as_float(__builtin_amdgcn_update_dpp(0, __float_as_int(v), CTRL, 0xf, 0xf, true)); }
DI float rowmax16(float v) {
  v = fmaxf(v, dppf<0xB1>(v)); v = fmaxf(v, dppf<0x4E>(v)); v = fmaxf(v, dppf<0x141>(v)); v = fmaxf(v, dppf<0x140>(v)); return v;
}

constexpr int EOFF = 143360;
constexpr int XOFF = EOFF + 17408;
constexpr int SYNC = XOFF + 2048;
constexpr int LDS_BYTES = SYNC + 256;

DI void signal_done(char* smem, unsigned* cnt, int lane, int nrep, int me) {
  wait_vm<0>();
  unsigned old = 0;
  if (lane == 0) old = __hip_atomic_fetch_add((LDSP(unsigned))(smem + SYNC), 1u, __ATOMIC_RELAXED, __HIP_MEMORY_SCOPE_WORKGROUP);
  if ((__builtin_amdgcn_readfirstlane(old) & 7u) == 7u && lane < nrep) {
    if (FLAGS) __hip_atomic_store((gu32*)(cnt + lane * 32 + me), 1u, __ATOMIC_RELAXED, __HIP_MEMORY_SCOPE_AGENT);
    else __hip_atomic_fetch_add((gu32*)(cnt + lane * 32), 1u, __ATOMIC_RELAXED, __HIP_MEMORY_SCOPE_AGENT);
  }
}

DI void lds_wait(char* smem, int off, unsigned v) {
  volatile unsigned* w = (volatile unsigned*)(smem + SYNC + off);
  for (unsigned spins = 0; spins < 8000000u; ++spins) {
    if ((unsigned)__builtin_amdgcn_readfirstlane(*w) >= v) break;
    __builtin_amdgcn_s_sleep(1);
  }
  asm volatile("" ::: "memory");
}

enum { EPI_QKV = 0, EPI_FFN1 = 1, EPI_RES = 2 };

template <int BN> struct BL {
  static constexpr bool PADDED = (BN == 96);
  static constexpr int RB = BN * 2;
  static constexpr int GS = PADDED ? 784 : 4 * RB;
  static constexpr int STAGE = 16 * GS;
  static constexpr int NI = PADDED ? 16 : (64 * RB / 1024);
  static DI int sw(int row) { return BN == 64 ? (((row >> 1) & 1) | (((row >> 3) & 1) << 1)) : ((row & 3) | (((row >> 3) & 1) << 2)); }
};

template <int NK, int NS, int P0, int L, int NE> struct Sched {
  static constexpr int mn(int a, int b) { return a < b ? a : b; }
  static constexpr int up(int j) { return mn(mn(P0 + 2 * j, (j < 1 ? 1 : j) + NS - 1), NK); }
  static constexpr int behind(int j) { return (up(j) - 1 - j) * L + (up(j) == NK ? NE : 0); }
};

template <int BM, int BN, int KTOT, int N, int NA, int NB, int PB, int SUB, int PD, int EPI, bool STI, bool FIRST>
DI void gemm_phase(char* smem, const u16* Ap, const u16* Bp, const u16* aux, u16* out, float* stats, int m0, int n0,
                   unsigned* wcnt, unsigned wtarget, unsigned* scnt, unsigned ph, unsigned narr, int rot0) {
  const int rot = ROT ? rot0 % (KTOT / (64 * SUB)) : 0;
  asm volatile("" : "+s"(Ap), "+s"(Bp), "+s"(aux), "+s"(out), "+s"(stats), "+s"(m0), "+s"(n0));
  constexpr int WM = 4, WN = 2;
  constexpr int NK = KTOT / (64 * SUB);
  constexpr int ASUB = BM * 128, BSUB = BL<BN>::STAGE;
  constexpr int ASTG = ASUB * SUB, BSTG = BSUB * SUB;
  constexpr int BOFF = NA * ASTG;
  static_assert(BOFF + NB * BSTG <= EOFF, "rings exceed LDS");
  constexpr int XCH = ((NK - 2) % NA) * ASTG;
  constexpr int MF = BM / WM / 16, NF = BN / WN / 16;
  constexpr int LA = BM / 16;
  constexpr int LB = BL<BN>::NI / 2;
  constexpr int LM = LA > LB ? LA : LB;
  constexpr bool LN = (EPI != EPI_RES);
  constexpr bool AHM = (EPI == EPI_RES && KTOT == 512);
  constexpr int NE = (EPI == EPI_RES) ? 2 : (STI ? 0 : (EPI == EPI_FFN1 ? 5 : 4));
  using SA = Sched<NK, NA, 2, LA * SUB, NE>;
  using SB = Sched<NK, NB, PB, LB * SUB, 0>;
  const unsigned lds0 = (unsigned)(size_t)(LDSP(char))smem;
  float* csb = (float*)(smem + XOFF);
  float* st = csb + 256;
  int tid = threadIdx.x;
  asm volatile("" : "+v"(tid));
  const int lane = tid & 63;
  const int wid = __builtin_amdgcn_readfirstlane(tid >> 6);
  const int l15 = lane & 15, g = lane >> 4;
  const int bq = l15 >> 2, bp = lane & 3;
  auto bfrag_off = [&](int f) -> unsigned {
    if (BL<BN>::PADDED) return (2 * g) * BL<BN>::GS + bq * BL<BN>::RB + f * 32 + bp * 8;
    const int row = 8 * g + bq;
    return row * BL<BN>::RB + ((f ^ BL<BN>::sw(row)) * 32) + bp * 8;
  };

  if (wid >= 8) {
    const int w4 = wid - 8;
    const bool isA = w4 < 2;
    const int w2 = w4 & 1;
    unsigned off[LM];
#pragma unroll
    for (int i = 0; i < LM; ++i) {
      if (isA) {
        const int ins = w2 * LA + i;
        const int row = ins * 8 + (lane >> 3), ch = (lane & 7) ^ ((row >> 1) & 7);
        off[i] = AHM ? (unsigned)((ch >> 1) * 65536 + row * 32 + (ch & 1) * 16) : (unsigned)(row * KTOT * 2 + ch * 16);
      } else {
        const int ins = w2 * LB + i;
        constexpr int CPR = BN / 8;
        if (BL<BN>::PADDED) {
          const int row = ins * 4 + lane / CPR, ch = lane % CPR;
          off[i] = (unsigned)(row * N * 2 + ch * 16);
        } else {
          const int row = ins * (64 / CPR) + lane / CPR;
          const int ch = (lane % CPR) ^ (BL<BN>::sw(row) << 1);
          off[i] = (unsigned)(row * N * 2 + ch * 16);
        }
      }
    }
    const char* abase = (const char*)Ap + (AHM ? (size_t)m0 * 32 : (size_t)m0 * KTOT * 2);
    const char* bbase = (const char*)Bp + (size_t)n0 * 2;
    auto extras = [&]() {
      if (EPI == EPI_RES) {
#pragma unroll
        for (int i = 0; i < 2; ++i) {
          const int row = (w4 * 2 + i) * 8 + (lane >> 3), ch = (lane & 7) ^ ((row >> 1) & 7);
          glds16s((const char*)aux + ((size_t)m0 * N + n0) * 2, (unsigned)(row * N * 2 + ch * 16), lds0 + EOFF + (w4 * 2 + i) * 1024);
        }
      } else if (!STI) {
#pragma unroll
        for (int i = 0; i < 4; ++i) glds16s((const char*)stats + (size_t)m0 * 128 + w4 * 256 + i * 4096, g * 1024 + l15 * 16, lds0 + EOFF + (w4 * 4 + i) * 1024);
        if (EPI == EPI_FFN1) glds16m<32>((const char*)aux + n0 * 2, (lane & 15) * 16, lds0 + EOFF + 16384);
      }
    };
    auto issueA = [&](int s) {
#pragma unroll
      for (int u = 0; u < SUB; ++u)
#pragma unroll
        for (int i = 0; i < LA; ++i)
          glds16s(abase + (size_t)(((s + rot >= NK) ? s + rot - NK : s + rot) * SUB + u) * (AHM ? 4 * 65536 : 128), off[i], lds0 + (s % NA) * ASTG + u * ASUB + (w2 * LA + i) * 1024);
      if (s == NK - 1) extras();
    };
    auto issueB = [&](int s) {
#pragma unroll
      for (int u = 0; u < SUB; ++u) {
        const unsigned slot = lds0 + BOFF + (s % NB) * BSTG + u * BSUB;
        const char* src = bbase + (size_t)(((s + rot >= NK) ? s + rot - NK : s + rot) * SUB + u) * 64 * N * 2;
#pragma unroll
        for (int i = 0; i < LB; ++i) {
          if (BL<BN>::PADDED) glds16m<BL<BN>::PADDED ? 4 * (BN / 8) : 64>(src, off[i], slot + (w2 * LB + i) * BL<BN>::GS);
          else glds16(src, off[i], slot + (w2 * LB + i) * 1024);
        }
      }
    };
    if (isA) {
      if (!FIRST) { if (LDPOLL) wait_cnt(wcnt, wtarget, lane); else lds_wait(smem, 8, ph); }
#pragma unroll
      for (int s = 0; s < SA::up(0); ++s) issueA(s);
    } else {
      if (!FIRST && !EARLYB) lds_wait(smem, 0, narr);
#pragma unroll
      for (int s = 0; s < SB::up(0); ++s) issueB(s);
    }
    const bf16x8 ONES = __builtin_bit_cast(bf16x8, u32x4{0x3f803f80u, 0x3f803f80u, 0x3f803f80u, 0x3f803f80u});
    const unsigned c_rd0 = lds0 + BOFF + bfrag_off(w4), c_rd1 = lds0 + BOFF + bfrag_off(w4 + 4);
    const unsigned a_rd = lds0 + (w4 * 32 + l15) * 128 + ((g ^ (l15 >> 1)) * 16);
    f32x4 cs0 = {0.f, 0.f, 0.f, 0.f}, cs1 = cs0, rs[2] = {cs0, cs0}, rq[2] = {cs0, cs0};
#pragma unroll
    for (int j = 0; j < NK; ++j) {
      if (isA) {
        wait_bar_n(SA::behind(j));
#pragma unroll
        for (int s = SA::up(j); s < SA::up(j + 1); ++s) issueA(s);
      } else {
        wait_bar_n(SB::behind(j) + ((j > NK - 3 + 0) && (NK - 2 >= 1) ? ((j >= NK - 1) ? NE : 0) : 0));
#pragma unroll
        for (int s = SB::up(j); s < SB::up(j + 1); ++s) issueB(s);
        if (j == NK - 2) extras();
      }
      if (LN) {
#pragma unroll
        for (int ks = 0; ks < 2; ++ks) {
          const unsigned o = (j % NB) * BSTG + ks * 8 * BL<BN>::GS;
          cs0 = __builtin_amdgcn_mfma_f32_16x16x32_bf16(CAT8(trread(c_rd0 + o), trread(c_rd0 + o + BL<BN>::GS)), ONES, cs0, 0, 0, 0);
          cs1 = __builtin_amdgcn_mfma_f32_16x16x32_bf16(CAT8(trread(c_rd1 + o), trread(c_rd1 + o + BL<BN>::GS)), ONES, cs1, 0, 0, 0);
          if (STI) {
#pragma unroll
            for (int mf = 0; mf < 2; ++mf) {
              const bf16x8 a = ldsread16((a_rd ^ (ks * 64)) + (j % NA) * ASTG + mf * 2048);
              rs[mf] = __builtin_amdgcn_mfma_f32_16x16x32_bf16(ONES, a, rs[mf], 0, 0, 0);
              rq[mf] = __builtin_amdgcn_mfma_f32_16x16x32_bf16(a, a, rq[mf], 0, 0, 0);
            }
          }
        }
        PIN(cs0); PIN(cs1);
        if (STI) { PIN(rs[0]); PIN(rs[1]); PIN(rq[0]); PIN(rq[1]); }
      }
    }
    if (LN) {
      if (l15 == 0) {
        *(f32x4*)(csb + w4 * 16 + g * 4) = cs0;
        *(f32x4*)(csb + 64 + w4 * 16 + g * 4) = cs1;
      }
      if (STI) {
#pragma unroll
        for (int mf = 0; mf < 2; ++mf) {
          const int jj = l15 & 3;
          const float d = (jj == 0) ? rq[mf][0] : (jj == 1) ? rq[mf][1] : (jj == 2) ? rq[mf][2] : rq[mf][3];
          if (g == (l15 >> 2)) *(f32x2*)(st + (w4 * 32 + mf * 16 + l15) * 2) = f32x2{rs[mf][0], d};
        }
      } else {
        wait_vm<0>();
        f32x4 v = *(const f32x4*)(smem + EOFF + (w4 * 4) * 1024 + lane * 16);
#pragma unroll
        for (int i = 1; i < 4; ++i) v += *(const f32x4*)(smem + EOFF + (w4 * 4 + i) * 1024 + lane * 16);
#pragma unroll
        for (int i = 0; i < 4; ++i) v[i] = sum32(sum16(v[i]));
        if (g == 0) *(f32x4*)(st + (w4 * 32 + 2 * l15) * 2) = v;
      }
    }
    wait_bar<0>();
    return;
  }

  constexpr bool KSP = (EPI == EPI_RES) && KSPLIT;
  constexpr int CMF = KSP ? 2 : MF;
  const int kh = KSP ? (wid >> 2) : 0;
  const int wn = KSP ? ((wid >> 1) & 1) : wid / WM;
  const int wm = KSP ? ((wid & 1) * 2 + kh) : wid % WM;
  const int trow = m0 + wm * (MF * 16) + l15;
  const int ncol = n0 + wn * NF * 16;
  const unsigned a_rd = lds0 + ((KSP ? (wid & 1) * 32 : wm * (MF * 16)) + l15) * 128 + ((g ^ (l15 >> 1)) * 16);
  unsigned b_rd[BL<BN>::PADDED ? 1 : NF];
#pragma unroll
  for (int nf = 0; nf < (BL<BN>::PADDED ? 1 : NF); ++nf) b_rd[nf] = lds0 + BOFF + bfrag_off(wn * NF + nf) + (KSP ? kh * 8 * BL<BN>::GS : 0);
  const unsigned a_rk = KSP ? (a_rd ^ (kh * 64)) : a_rd;
  f32x4 acc[CMF][NF];
#pragma unroll
  for (int a = 0; a < CMF; ++a)
#pragma unroll
    for (int b = 0; b < NF; ++b) acc[a][b] = f32x4{0.f, 0.f, 0.f, 0.f};

  constexpr int KS = KSP ? SUB : 2 * SUB, GPS = KS / PD, NG = NK * GPS;
  bf16x8 af[2][PD][CMF], bfr[2][PD][NF];
  auto rdfrags = [&](int gi) {
    const int pb = gi & 1, s = gi / GPS;
#pragma unroll
    for (int q = 0; q < PD; ++q) {
      const int k = KSP ? 2 * ((gi % GPS) * PD + q) : (gi % GPS) * PD + q;
#pragma unroll
      for (int mf = 0; mf < CMF; ++mf) af[pb][q][mf] = ldsread16((a_rk ^ ((k & 1) * 64)) + (s % NA) * ASTG + (k >> 1) * ASUB + mf * 2048);
#pragma unroll
      for (int nf = 0; nf < NF; ++nf) {
        const unsigned o = (BL<BN>::PADDED ? b_rd[0] + nf * 32 : b_rd[nf]) + (s % NB) * BSTG + (k >> 1) * BSUB + (k & 1) * 8 * BL<BN>::GS;
        bfr[pb][q][nf] = CAT8(trread(o), trread(o + BL<BN>::GS));
      }
    }
  };
  if (!FIRST && !LDPOLL && wid < NPOLL) {
    if (wid) __builtin_amdgcn_s_sleep(6);
    wait_cnt(wcnt, wtarget, lane); acquire_cu(); *(volatile unsigned*)(smem + SYNC + 8) = ph;
  }
  lds_bar();
  rdfrags(0);
#pragma unroll
  for (int gi = 0; gi < NG; ++gi) {
    if (gi + 1 < NG) {
      if ((gi + 1) % GPS == 0) lds_bar();
      SBAR();
      rdfrags(gi + 1);
    }
    const int pb = gi & 1;
#pragma unroll
    for (int q = 0; q < PD; ++q)
#pragma unroll
      for (int nf = 0; nf < NF; ++nf)
#pragma unroll
        for (int mf = 0; mf < CMF; ++mf)
          acc[mf][nf] = __builtin_amdgcn_mfma_f32_16x16x32_bf16(bfr[pb][q][nf], af[pb][q][mf], acc[mf][nf], 0, 0, 0);
    if (gi + 1 < NG) {
      constexpr int NM = PD * CMF * NF, ND = PD * (CMF + 2 * NF);
      constexpr int PER = (ND + NM - 1) / NM;
#pragma unroll
      for (int i = 0; i < NM; ++i) {
        __builtin_amdgcn_sched_group_barrier(0x100, PER, 0);
        __builtin_amdgcn_sched_group_barrier(0x8, 1, 0);
      }
    }
#pragma unroll
    for (int mf = 0; mf < CMF; ++mf)
#pragma unroll
      for (int nf = 0; nf < NF; ++nf) PIN(acc[mf][nf]);
  }
  if (KSP) {
#pragma unroll
    for (int nf = 0; nf < NF; ++nf) *(f32x4*)(smem + XCH + (wid * NF + nf) * 1024 + lane * 16) = kh ? acc[0][nf] : acc[CMF - 1][nf];
  }
  lds_bar();
  if (KSP) {
#pragma unroll
    for (int nf = 0; nf < NF; ++nf) acc[0][nf] = (kh ? acc[CMF - 1][nf] : acc[0][nf]) + *(const f32x4*)(smem + XCH + ((wid ^ 4) * NF + nf) * 1024 + lane * 16);
  }
  if (LN) {
    f32x4 c0[NF];
    float rstd[MF], nmr[MF];
#pragma unroll
    for (int nf = 0; nf < NF; ++nf) c0[nf] = *(const f32x4*)(csb + (wn * NF + nf) * 16 + g * 4);
#pragma unroll
    for (int mf = 0; mf < MF; ++mf) {
      const f32x2 a = *(const f32x2*)(st + (wm * (MF * 16) + mf * 16 + l15) * 2);
      const float mean = a[0] * (1.f / KTOT);
      const float var = fmaxf(a[1] * (1.f / KTOT) - mean * mean, 0.f);
      rstd[mf] = __builtin_amdgcn_rsqf(var + 1e-5f);
      nmr[mf] = -mean * rstd[mf];
    }
    if (EPI == EPI_QKV) {
      const int tk = trow + (g & 1) * 16;
      const unsigned lin = tk * 16 + (g >> 1) * 8;
      const unsigned kof = (tk >> 5) * 512 + (g >> 1) * 256 + (tk & 31) * 8;
#pragma unroll
      for (int nf = 0; nf < NF; ++nf) {
        const int hh = (ncol >> 4) + nf;
        const bool isq = hh < 32, isk = (hh >> 5) == 1;
        const float sc = isq ? 0.25f * 1.4426950408889634f : 1.f;
        u16* base = out + (size_t)hh * (SEQ * 16);
        u32x2 o[MF];
#pragma unroll
        for (int mf = 0; mf < MF; ++mf) {
          const f32x4 y = acc[mf][nf] * (rstd[mf] * sc) + c0[nf] * (nmr[mf] * sc);
          o[mf] = u32x2{cvtpk(y[0], y[1]), cvtpk(y[2], y[3])};
        }
        st16(base + (isk ? kof : lin), pair16(o[0], o[MF - 1]));
      }
    } else {
      u32x2 o[MF][NF];
#pragma unroll
      for (int nf = 0; nf < NF; ++nf) {
        const u32x2 bb = *(const u32x2*)(smem + EOFF + 16384 + ((wn * NF + nf) * 16 + g * 4) * 2);
        const f32x4 bias = {bflo(bb[0]), bfhi(bb[0]), bflo(bb[1]), bfhi(bb[1])};
#pragma unroll
        for (int mf = 0; mf < MF; ++mf) {
          const f32x4 h = acc[mf][nf] * rstd[mf] + (c0[nf] * nmr[mf] + bias);
          const f32x4 t = h * 1.4426950408889634f;
          f32x4 u, rc;
#pragma unroll
          for (int j = 0; j < 4; ++j) u[j] = __builtin_amdgcn_exp2f(t[j]);
          const f32x4 d = u * (u * -0.5f - 1.f) - 1.f;
#pragma unroll
          for (int j = 0; j < 4; ++j) rc[j] = __builtin_amdgcn_rcpf(d[j]);
          const f32x4 a = h * rc + h;
          o[mf][nf] = u32x2{cvtpk(a[0], a[1]), cvtpk(a[2], a[3])};
          if (nf & 1) st16pair(out + (size_t)(trow + mf * 16) * N + ncol + (nf - 1) * 16, g, o[mf][nf - 1], o[mf][nf]);
        }
      }
    }
  } else {
#pragma unroll
    for (int mf = 0; mf < MF; ++mf) {
      const int tok = trow + mf * 16;
      float s1 = 0.f, s2 = 0.f;
      u32x2 o[NF];
#pragma unroll
      for (int nf = 0; nf < NF; ++nf) {
        const u32x2 rr = *(const u32x2*)(smem + EOFF + (wm * (MF * 16) + mf * 16 + l15) * 128 + ((((wn * NF + nf) * 2 + (g >> 1)) ^ (l15 >> 1)) * 16) + (g & 1) * 8);
        const f32x4 r = acc[mf][nf];
        o[nf] = u32x2{cvtpk(r[0] + bflo(rr[0]), r[1] + bfhi(rr[0])), cvtpk(r[2] + bflo(rr[1]), r[3] + bfhi(rr[1]))};
        const float e0 = bflo(o[nf][0]), e1 = bfhi(o[nf][0]), e2 = bflo(o[nf][1]), e3 = bfhi(o[nf][1]);
        s1 += (e0 + e1) + (e2 + e3);
        s2 += (e0 * e0 + e1 * e1) + (e2 * e2 + e3 * e3);
        if (nf & 1) st16pair(out + (size_t)tok * N + ncol + (nf - 1) * 16, g, o[nf - 1], o[nf]);
      }
      s1 = sum32(sum16(s1)); s2 = sum32(sum16(s2));
      if (g == 0) st8(stats + (tok >> 7) * 4096 + (ncol >> 5) * 256 + (tok & 127) * 2, f32x2{s1, s2});
    }
  }
  if (!FIRST) signal_done(smem, scnt, lane, EPI == EPI_QKV ? 32 : 16, EPI == EPI_QKV ? (m0 >> 7) : ((blockIdx.x >> 3) & 15));
}

constexpr int AT_K = 0, AT_V = 65536, AT_ONE = 131072, AT_Q = AT_ONE + 6144, AT_PF = AT_Q + 8 * 9 * 256;
static_assert(AT_PF + 1024 <= XOFF, "attention LDS");

#define ATT_HALF(SC, OO, VC, MID)                                                                            \
  {                                                                                                          \
    _Pragma("unroll") for (int i = 0; i < 4; ++i) SC[i] = __builtin_amdgcn_exp2f(SC[i]);                     \
    SBAR(); MID; SBAR();                                                                                     \
    _Pragma("unroll") for (int i = 4; i < 9; ++i) SC[i] = __builtin_amdgcn_exp2f(SC[i]);                     \
    u32x4 p0 = {cvtpk(SC[0], SC[1]), cvtpk(SC[2], SC[3]), cvtpk(SC[4], SC[5]), cvtpk(SC[6], SC[7])};         \
    PIN(p0); SBAR();                                                                                         \
    SC[9] = __builtin_amdgcn_exp2f(SC[9]); SBAR();                                                           \
    OO = MFMA32(CAT8(VC[0], VC[1]), __builtin_bit_cast(bf16x8, p0), OO); SBAR();                             \
    _Pragma("unroll") for (int i = 10; i < 16; ++i) SC[i] = __builtin_amdgcn_exp2f(SC[i]);                   \
    u32x4 p1 = {cvtpk(SC[8], SC[9]), cvtpk(SC[10], SC[11]), cvtpk(SC[12], SC[13]), cvtpk(SC[14], SC[15])};   \
    OO = MFMA32(CAT8(VC[2], VC[3]), __builtin_bit_cast(bf16x8, p1), OO); SBAR();                             \
  }
#define ATT_TILE(VC, VN, T)                                                                                  \
  ATT_HALF(s0, o0, VC, { s1 = MFMA32(kf, qf1, cm1); PIN(s1); kf = ldsread16(kaddr + ((T) + 1) * 1024);       \
       VN[0] = trread(vaddr + ((T) + 1) * 1024); VN[1] = trread(vaddr + ((T) + 1) * 1024 + 256); })          \
  ATT_HALF(s1, o1, VC, { s0 = MFMA32(kf, qf0, cm0); PIN(s0);                                                 \
       VN[2] = trread(vaddr + ((T) + 1) * 1024 + 512); VN[3] = trread(vaddr + ((T) + 1) * 1024 + 768); })

template <bool FAST>
DI void attn_pass(unsigned lds0, int lane, int ks, const bf16x8 qf0, const bf16x8 qf1, float nm0, float nm1, f32x16& o0, f32x16& o1) {
  const int h = lane >> 5;
  unsigned kaddr = lds0 + AT_K + ks * 32768 + h * 512 + (lane & 31) * 16;
  const bool cl = (lane >> 4) & 1;
  const int qq = (lane & 15) >> 2, pp = lane & 3;
  unsigned vaddr = cl ? lds0 + AT_ONE + (4 * (h ^ 1) + qq) * 32 + pp * 8 : lds0 + AT_V + ks * 32768 + (4 * h + qq) * 32 + pp * 8;
  const unsigned vstep = cl ? 0u : 4096u;
  f32x16 cm0, cm1, s0, s1;
  s16x4 vA[4], vB[4];
#pragma unroll
  for (int i = 0; i < 16; ++i) { cm0[i] = FAST ? 0.f : nm0; cm1[i] = FAST ? 0.f : nm1; o0[i] = 0.f; o1[i] = 0.f; }
  bf16x8 kf = ldsread16(kaddr);
  s0 = MFMA32(kf, qf0, cm0);
  vA[0] = trread(vaddr); vA[1] = trread(vaddr + 256); vA[2] = trread(vaddr + 512); vA[3] = trread(vaddr + 768);
  for (int it = 0; it < 8; ++it) {
    PIN(kaddr); PIN(vaddr);
    ATT_TILE(vA, vB, 0) ATT_TILE(vB, vA, 1) ATT_TILE(vA, vB, 2) ATT_TILE(vB, vA, 3)
    kaddr += 4096; vaddr += vstep;
    lds_bar();
  }
}

DI void attn_phase(char* smem, const u16* QKV, u16* O, const u16* wo, const u16* w1, const u16* w2, unsigned* cq, bool wait, unsigned* scnt) {
  asm volatile("" : "+s"(QKV), "+s"(O));
  const unsigned lds0 = (unsigned)(size_t)(LDSP(char))smem;
  int tid = threadIdx.x;
  asm volatile("" : "+v"(tid));
  const int lane = tid & 63;
  const int wid = __builtin_amdgcn_readfirstlane(tid >> 6);
  const int bid = blockIdx.x;
  const int head = (bid & 7) * 4 + ((bid >> 3) & 3), qb = bid >> 5;
  volatile unsigned* flag = (volatile unsigned*)(smem + SYNC + 4);
  if (wid >= 8) {
    const int w4 = wid - 8;
    const unsigned l16 = lane * 16;
#pragma unroll
    for (int i = 0; i < 2; ++i) { const int t = (w4 * 2 + i) * 64 + lane; if (t < 384) *(u32x4*)(smem + AT_ONE + t * 16) = u32x4{(t & 1) ? 0u : 0x3f80u, 0u, 0u, 0u}; }
    if (tid == 767) *flag = 0u;
    if (wait) {
      wait_cnt(cq + ((head / 6) * 32 + qb * 4 + w4) * 32, 16, lane); wait_cnt(cq + (((32 + 32 * (w4 >> 1) + head) / 6) * 32 + qb * 4 + w4) * 32, 16, lane);
      acquire_cu();
    }
    const char* qsrc = (const char*)QKV + ((size_t)head * SEQ + qb * 256) * 32 + w4 * 2048;
    const char* src = (const char*)QKV + (size_t)(32 + 32 * (w4 >> 1) + head) * 65536 + (w4 & 1) * 32768;
    const unsigned dst = lds0 + (w4 >> 1) * 65536 + (w4 & 1) * 32768;
    glds16s(qsrc, l16, lds0 + AT_Q + w4 * 2048); glds16s(qsrc + 1024, l16, lds0 + AT_Q + w4 * 2048 + 1024);
#pragma unroll
    for (int i = 0; i < 8; ++i) glds16s(src + i * 1024, l16, dst + i * 1024);
    wait_bar<0>();
#pragma unroll
    for (int i = 8; i < 32; ++i) glds16s(src + i * 1024, l16, dst + i * 1024);
    wait_bar<0>();
    {
      const int u = (bid >> 3) * 4 + w4;
      const char* p1 = (u < 28) ? (const char*)wo + u * 18432 : (const char*)w1 + (u - 28) * 18432;
      const int n1 = (u < 28) ? (u == 27 ? 26624 : 18432) : (u == 127 ? 10240 : 18432);
      const char* p2 = (const char*)w2 + (size_t)(bid * 4 + w4) * 1792;
      int off = 0;
      for (int it = 1; it < 8; ++it) {
#pragma unroll
        for (int i = 0; i < 4; ++i) { if (off < n1) glds16(p1 + off, l16, lds0 + AT_PF); off += 1024; }
        if (it == 1) { glds16(p2, l16, lds0 + AT_PF); glds16m<48>(p2 + 1024, l16, lds0 + AT_PF); }
        if (it == 7) wait_bar<0>(); else lds_bar();
      }
    }
    lds_bar();
    if (__builtin_amdgcn_readfirstlane(*flag) != 0u) {
      for (int it = 0; it < 9; ++it) lds_bar();
    }
    return;
  }
  const int wq = wid & 3, ks = wid >> 2;
  lds_bar();
  const bf16x8 qf0 = ldsread16(lds0 + AT_Q + wq * 2048 + (lane & 31) * 32 + (lane >> 5) * 16);
  const bf16x8 qf1 = ldsread16(lds0 + AT_Q + wq * 2048 + 1024 + (lane & 31) * 32 + (lane >> 5) * 16);
  f32x16 o0, o1;
  attn_pass<true>(lds0, lane, ks, qf0, qf1, 0.f, 0.f, o0, o1);
  float* xb = (float*)(smem + AT_Q);
  if (lane < 32 && !(o0[8] >= 8.67e-19f && o0[8] <= 1.26e30f && o1[8] >= 8.67e-19f && o1[8] <= 1.26e30f)) *flag = 1u;
#pragma unroll
  for (int i = 0; i < 9; ++i) xb[(wid * 9 + i) * 64 + lane] = ks ? o0[i] : o1[i];
  lds_bar();
  if (__builtin_amdgcn_readfirstlane(*flag) != 0u) {
    float m0 = -3.0e38f, m1 = -3.0e38f;
    const unsigned kaddr = lds0 + AT_K + (lane >> 5) * 512 + (lane & 31) * 16;
    f32x16 z;
#pragma unroll
    for (int i = 0; i < 16; ++i) z[i] = 0.f;
    for (int t = 0; t < 64; ++t) {
      const bf16x8 kf = ldsread16(kaddr + t * 1024);
      const f32x16 sa = MFMA32(kf, qf0, z), sb = MFMA32(kf, qf1, z);
#pragma unroll
      for (int i = 0; i < 16; ++i) { m0 = fmaxf(m0, sa[i]); m1 = fmaxf(m1, sb[i]); }
    }
    m0 = max32(m0); m1 = max32(m1);
    attn_pass<false>(lds0, lane, ks, qf0, qf1, -m0, -m1, o0, o1);
#pragma unroll
    for (int i = 0; i < 9; ++i) xb[(wid * 9 + i) * 64 + lane] = ks ? o0[i] : o1[i];
    lds_bar();
  }
  f32x16 o = ks ? o1 : o0;
#pragma unroll
  for (int i = 0; i < 9; ++i) o[i] += xb[((wid ^ 4) * 9 + i) * 64 + lane];
  auto lr = __builtin_amdgcn_permlane32_swap(__float_as_uint(o[8]), __float_as_uint(o[8]), false, false);
  const float rl = __builtin_amdgcn_rcpf(__uint_as_float(lr[0]));
  unsigned x0 = cvtpk(o[0] * rl, o[1] * rl), x1 = cvtpk(o[2] * rl, o[3] * rl);
  unsigned y0 = cvtpk(o[4] * rl, o[5] * rl), y1 = cvtpk(o[6] * rl, o[7] * rl);
  auto r0 = __builtin_amdgcn_permlane32_swap(x0, y0, false, false);
  auto r1 = __builtin_amdgcn_permlane32_swap(x1, y1, false, false);
  const int q0 = qb * 256 + wq * 64 + ks * 32;
  st16(O + ((size_t)head * SEQ + q0 + (lane & 31)) * 16 + (lane >> 5) * 8, u32x4{r0[0], r1[0], r0[1], r1[1]});
  signal_done(smem, scnt, lane, 32, bid & 31);
}

constexpr size_t MB = 1 << 20;
constexpr size_t WS_QKV = 0, WS_O = 12 * MB, WS_X1 = 16 * MB, WS_X2 = 20 * MB, WS_H = 22 * MB, WS_ST = 38 * MB, WS_CNT = 39 * MB;
constexpr int C_A = 0, C_1 = 256, C_2 = 512, C_3 = 768, C_LAYER = 1024, C_Q = 2048, C_TOTAL = 2560;

struct Params {
  const u16 *x, *wqkv, *wo, *w1, *b1, *w2;
  u16* out;
  char* ws;
};

extern "C" __global__ __launch_bounds__(768, 3) void k_mega(Params p) {
  __shared__ __attribute__((aligned(256))) char smem[LDS_BYTES];
  if (threadIdx.x == 0) { *(unsigned*)(smem + SYNC) = 0u; *(unsigned*)(smem + SYNC + 8) = 0u; }
  if (__builtin_amdgcn_readfirstlane(threadIdx.x) >= 512) __builtin_amdgcn_s_setprio(3);
  const int bid = blockIdx.x, idx = bid >> 3;
  const int team = (bid & 7) * 2 + (idx >> 4), mem = idx & 15;
  unsigned narr = 0;
  for (int layer = 0; layer < 2; ++layer) {
    asm volatile("" : "+s"(p.ws));
    unsigned* cnt = (unsigned*)(p.ws + WS_CNT);
    unsigned* cl = cnt + layer * C_LAYER * 32;
    u16* O = (u16*)(p.ws + WS_O + layer * 2 * MB);
    u16* X1 = (u16*)(p.ws + WS_X1 + layer * 2 * MB);
    u16* X2 = (u16*)(p.ws + WS_X2);
    u16* H = (u16*)(p.ws + WS_H + layer * 8 * MB);
    float* st1 = (float*)(p.ws + WS_ST + layer * 2 * 262144);
    float* st2 = st1 + 65536;
    attn_phase(smem, (const u16*)(p.ws + WS_QKV + layer * 6 * MB), O, p.wo, p.w1, p.w2, cnt + C_Q * 32, layer != 0, cl + (C_A + (bid >> 5) * 32) * 32);
    narr += 8;
    gemm_phase<64, 64, 512, 512, CFG_O, EPI_RES, true, false>(smem, O, p.wo, layer ? X2 : p.x, X1, st1, team * 128 + (mem >> 3) * 64, (mem & 7) * 64,
                                                                    cl + (C_A + team * 16 + mem) * 32, 32, cl + (C_1 + team * 16) * 32, 1 + layer * 8, narr, mem + team);
    narr += 8;
    if (mem < 14) {
      gemm_phase<128, 128, 512, 1792, CFG_1, EPI_FFN1, false, false>(smem, X1, p.w1, p.b1, H, st1, team * 128, mem * 128,
                                                                          cl + (C_1 + team * 16 + mem) * 32, 16, cl + (C_2 + team * 16) * 32, 2 + layer * 8, narr, mem + team * 4);
      narr += 8;
    }
    gemm_phase<64, 64, 1792, 512, CFG_2, EPI_RES, true, false>(smem, H, p.w2, X1, layer ? p.out : X2, st2, team * 128 + (mem >> 3) * 64, (mem & 7) * 64,
                                                                     cl + (C_2 + team * 16 + mem) * 32, 14, cl + (C_3 + team * 16) * 32, 3 + layer * 8, narr, (mem & 7) * 2 + (mem >> 3) + (team & 1) * 7);
    narr += 8;
    if (layer == 0) {
      gemm_phase<128, 96, 512, 1536, CFG_Q, EPI_QKV, false, false>(smem, X2, p.wqkv, nullptr, (u16*)(p.ws + WS_QKV + 6 * MB), st2, team * 128, mem * 96,
                                                                        cl + (C_3 + team * 16 + mem) * 32, 16, cnt + (C_Q + mem * 32) * 32, 4, narr, mem + team * 4);
      narr += 8;
    }
  }
}
extern "C" __global__ void k_zend() {}

extern "C" __global__ __launch_bounds__(768, 3) void k_qkv1(Params p) {
  __shared__ __attribute__((aligned(256))) char smem[LDS_BYTES];
  const int bid = blockIdx.x, xcd = bid & 7, idx = bid >> 3;
  if (__builtin_amdgcn_readfirstlane(threadIdx.x) >= 512) __builtin_amdgcn_s_setprio(3);
  if (threadIdx.x < C_TOTAL / 32) *(u32x4*)(p.ws + WS_CNT + (bid * (C_TOTAL / 32) + threadIdx.x) * 16) = u32x4{0u, 0u, 0u, 0u};
  unsigned codev = 0;
  {
    unsigned long long lo, hi;
    asm volatile("s_getpc_b64 s[20:21]\n\ts_add_u32 s20, s20, k_mega@rel32@lo+4\n\ts_addc_u32 s21, s21, k_mega@rel32@hi+12\n\ts_mov_b64 %0, s[20:21]\n\t"
                 "s_getpc_b64 s[20:21]\n\ts_add_u32 s20, s20, k_zend@rel32@lo+4\n\ts_addc_u32 s21, s21, k_zend@rel32@hi+12\n\ts_mov_b64 %1, s[20:21]"
                 : "=s"(lo), "=s"(hi)::"s20", "s21", "scc");
    const unsigned long long a = lo + (unsigned long long)(idx * 2048 + threadIdx.x * 4);
    if (threadIdx.x < 512 && a + 4 <= hi) codev = *(const __attribute__((address_space(1))) unsigned*)a;
  }
  unsigned touch = 0;
  if (TOUCH && threadIdx.x < 512) {
    const unsigned t = bid * 512 + threadIdx.x;
    if (t < 32768) touch = *(const unsigned*)((const char*)p.x + (size_t)t * 64);
    else if (t < 57344) touch = *(const unsigned*)((const char*)p.wqkv + (size_t)(t - 32768) * 64);
  }
  gemm_phase<128, 96, 512, 1536, CFG_F, EPI_QKV, true, true>(smem, p.x, p.wqkv, nullptr, (u16*)(p.ws + WS_QKV), nullptr,
                                                                   ((xcd >> 1) * 4 + idx / 8) * 128, ((xcd & 1) * 8 + idx % 8) * 96, nullptr, 0, nullptr, 0, 0, idx + (idx >> 3));
  asm volatile("" ::"v"(codev), "v"(touch));
}

extern "C" void kernel_launch(void* const* d_in, const int* in_sizes, int n_in, void* d_out, int out_size, void* d_ws,
                              size_t ws_size, hipStream_t stream) {
  Params p;
  p.x = (const u16*)d_in[0]; p.wqkv = (const u16*)d_in[1]; p.wo = (const u16*)d_in[2];
  p.w1 = (const u16*)d_in[4]; p.b1 = (const u16*)d_in[5]; p.w2 = (const u16*)d_in[6];
  p.out = (u16*)d_out; p.ws = (char*)d_ws;
  k_qkv1<<<256, 768, 0, stream>>>(p);
  k_mega<<<256, 768, 0, stream>>>(p);
}
```

```cpp
#include <hip/hip_runtime.h>
#include <hip/hip_bf16.h>
#include <stdint.h>

typedef short bf16x8 __attribute__((ext_vector_type(8)));
typedef short s16x4 __attribute__((ext_vector_type(4)));
typedef __bf16 bf16x2 __attribute__((ext_vector_type(2)));
typedef float f32x4 __attribute__((ext_vector_type(4)));
typedef float f32x16 __attribute__((ext_vector_type(16)));
typedef unsigned u32x2 __attribute__((ext_vector_type(2)));
typedef unsigned u32x4 __attribute__((ext_vector_type(4)));
typedef unsigned short u16;
#define LDSP(T) __attribute__((address_space(3))) T*
#define DI __device__ __forceinline__

#define NOCP 0
#define POLLSLEEP 0
#define EARLYB 0
#define PB_O 3
#define PB_1 3
#define PB_2 3
#define PB_Q 4
#define SC1LD 1
#define ROT 0
#define CFG_O 4, 4, 3, 2, 2
#define CFG_1 3, 5, 3, 1, 2
#define CFG_2 3, 5, 3, 2, 2
#define CFG_Q 3, 7, 4, 1, 2
#define CFG_F 3, 3, 2, 1, 2
#define KSPLIT 0
#define TOUCH 0
#define NPOLL 1
#define FLAGS 0
#define LDPOLL 0
#define ATBAR 0
#define ATPRIO 0
#define CSREUSE 1
#define W2WARM 0
#define TLBW 0
#define ABL 0
constexpr int SEQ = 2048, DM = 512, NH = 32, DH = 16, FF = 1792;

typedef float f32x2 __attribute__((ext_vector_type(2)));
DI unsigned cvtpk(float lo, float hi) { return __builtin_bit_cast(unsigned, __builtin_convertvector(f32x2{lo, hi}, bf16x2)); }
DI float bflo(unsigned u) { return __uint_as_float(u << 16); }
DI float bfhi(unsigned u) { return __uint_as_float(u & 0xffff0000u); }
DI float dot2(unsigned a, unsigned b, float c) {
  return __builtin_amdgcn_fdot2_f32_bf16(__builtin_bit_cast(bf16x2, a), __builtin_bit_cast(bf16x2, b), c, false);
}
DI void glds16(const void* sbase, unsigned voff, unsigned lds_addr) {
  unsigned sv;
  asm volatile("s_mov_b32 %0, m0\n\ts_mov_b32 m0, %3\n\ts_nop 0\n\tglobal_load_lds_dwordx4 %1, %2\n\ts_mov_b32 m0, %0"
               : "=&s"(sv) : "v"(voff), "s"(sbase), "s"(lds_addr) : "memory");
}
DI void glds16s(const void* sbase, unsigned voff, unsigned lds_addr) {
  if (!SC1LD) { glds16(sbase, voff, lds_addr); return; }
  unsigned sv;
  asm volatile("s_mov_b32 %0, m0\n\ts_mov_b32 m0, %3\n\ts_nop 0\n\tglobal_load_lds_dwordx4 %1, %2 sc1\n\ts_mov_b32 m0, %0"
               : "=&s"(sv) : "v"(voff), "s"(sbase), "s"(lds_addr) : "memory");
}
template <int NL> DI void glds16m(const void* sbase, unsigned voff, unsigned lds_addr) {
  unsigned sv;
  asm volatile("s_mov_b32 %0, m0\n\ts_mov_b32 m0, %3\n\ts_mov_b32 exec_hi, %4\n\tglobal_load_lds_dwordx4 %1, %2\n\ts_mov_b32 exec_hi, -1\n\ts_mov_b32 m0, %0"
               : "=&s"(sv) : "v"(voff), "s"(sbase), "s"(lds_addr), "i"((unsigned)((1ull << (NL - 32)) - 1)) : "memory");
}
#define SBAR() __builtin_amdgcn_sched_barrier(0)
#define PIN(x) asm volatile("" : "+v"(x))
#define MFMA32(a, b, c) __builtin_amdgcn_mfma_f32_32x32x16_bf16(a, b, c, 0, 0, 0)
#define CAT8(a, b) __builtin_shufflevector(a, b, 0, 1, 2, 3, 4, 5, 6, 7)
template <int N> DI void wait_bar() { asm volatile("s_waitcnt vmcnt(%0) lgkmcnt(0)\n\ts_barrier" ::"i"(N) : "memory"); }
DI void wait_bar_n(int n) { asm volatile("s_waitcnt vmcnt(%0) lgkmcnt(0)\n\ts_barrier" ::"i"(n) : "memory"); }
template <int N> DI void wait_vm() { asm volatile("s_waitcnt vmcnt(%0)" ::"i"(N) : "memory"); }
DI void lds_bar() { asm volatile("s_waitcnt lgkmcnt(0)\n\ts_barrier" ::: "memory"); }
#define STF 1
DI void st16(void* ptr, u32x4 v) {
  if (ABL & 32) { PIN(v); return; }
  if (STF == 0) *(u32x4*)ptr = v;
  else asm volatile("global_store_dwordx4 %0, %1, off sc1\n\ts_nop 1" ::"v"(ptr), "v"(v) : "memory");
}
DI u32x4 pair16(u32x2 a, u32x2 b) {
  auto r0 = __builtin_amdgcn_permlane16_swap(a[0], b[0], false, false);
  auto r1 = __builtin_amdgcn_permlane16_swap(a[1], b[1], false, false);
  return u32x4{r0[0], r1[0], r0[1], r1[1]};
}
DI void st16pair(u16* rowp, int g, u32x2 a, u32x2 b) { st16(rowp + (g & 1) * 16 + (g >> 1) * 8, pair16(a, b)); }
DI void st8(void* ptr, f32x2 v) { asm volatile("global_store_dwordx2 %0, %1, off sc1" ::"v"(ptr), "v"(v) : "memory"); }
typedef __attribute__((address_space(1))) unsigned gu32;
DI f32x2 ld8s(const void* p) {
  return __builtin_bit_cast(f32x2, __hip_atomic_load((const __attribute__((address_space(1))) unsigned long long*)p, __ATOMIC_RELAXED, __HIP_MEMORY_SCOPE_AGENT));
}
DI void wait_cnt(unsigned* c, unsigned target, int lane) {
  for (unsigned spins = 0; spins < 200000u; ++spins) {
    if (FLAGS) {
      unsigned v = 1;
      if (lane < (int)target) v = __hip_atomic_load((gu32*)(c + lane), __ATOMIC_RELAXED, __HIP_MEMORY_SCOPE_AGENT);
      if (__all(v != 0u)) break;
    } else {
      unsigned v = 0;
      if (lane == 0) v = __hip_atomic_load((gu32*)c, __ATOMIC_RELAXED, __HIP_MEMORY_SCOPE_AGENT);
      if ((unsigned)__builtin_amdgcn_readfirstlane(v) >= target) break;
    }
    if (POLLSLEEP) __builtin_amdgcn_s_sleep(POLLSLEEP);
  }
  asm volatile("" ::: "memory");
}
DI void acquire_cu() {
  if (SC1LD) return;
  __builtin_amdgcn_fence(__ATOMIC_ACQUIRE, "agent");
  asm volatile("s_waitcnt vmcnt(0)" ::: "memory");
}
DI s16x4 trread(unsigned addr) {
  return __builtin_bit_cast(s16x4, __builtin_amdgcn_ds_read_tr16_b64_v4i16((LDSP(s16x4))(size_t)addr));
}
DI bf16x8 ldsread16(unsigned addr) { return *(const LDSP(bf16x8))(size_t)addr; }
DI float sum16(float v) { auto r = __builtin_amdgcn_permlane16_swap(__float_as_uint(v), __float_as_uint(v), false, false); return __uint_as_float(r[0]) + __uint_as_float(r[1]); }
DI float sum32(float v) { auto r = __builtin_amdgcn_permlane32_swap(__float_as_uint(v), __float_as_uint(v), false, false); return __uint_as_float(r[0]) + __uint_as_float(r[1]); }
DI float max16(float v) { auto r = __builtin_amdgcn_permlane16_swap(__float_as_uint(v), __float_as_uint(v), false, false); return fmaxf(__uint_as_float(r[0]), __uint_as_float(r[1])); }
DI float max32(float v) { auto r = __builtin_amdgcn_permlane32_swap(__float_as_uint(v), __float_as_uint(v), false, false); return fmaxf(__uint_as_float(r[0]), __uint_as_float(r[1])); }
template <int CTRL> DI float dppf(float v) { return __int_as_float(__builtin_amdgcn_update_dpp(0, __float_as_int(v), CTRL, 0xf, 0xf, true)); }
DI float rowmax16(float v) {
  v = fmaxf(v, dppf<0xB1>(v)); v = fmaxf(v, dppf<0x4E>(v)); v = fmaxf(v, dppf<0x141>(v)); v = fmaxf(v, dppf<0x140>(v)); return v;
}

constexpr int EOFF = 143360;
constexpr int XOFF = EOFF + 17408;
constexpr int SYNC = XOFF + 2048;
constexpr int LDS_BYTES = SYNC + 256;

DI void signal_done(char* smem, unsigned* cnt, int lane, int nrep, int me) {
  wait_vm<0>();
  unsigned old = 0;
  if (lane == 0) old = __hip_atomic_fetch_add((LDSP(unsigned))(smem + SYNC), 1u, __ATOMIC_RELAXED, __HIP_MEMORY_SCOPE_WORKGROUP);
  if ((__builtin_amdgcn_readfirstlane(old) & 7u) == 7u && lane < nrep) {
    if (FLAGS) __hip_atomic_store((gu32*)(cnt + lane * 32 + me), 1u, __ATOMIC_RELAXED, __HIP_MEMORY_SCOPE_AGENT);
    else __hip_atomic_fetch_add((gu32*)(cnt + lane * 32), 1u, __ATOMIC_RELAXED, __HIP_MEMORY_SCOPE_AGENT);
  }
}

DI void lds_wait(char* smem, int off, unsigned v) {
  volatile unsigned* w = (volatile unsigned*)(smem + SYNC + off);
  for (unsigned spins = 0; spins < 8000000u; ++spins) {
    if ((unsigned)__builtin_amdgcn_readfirstlane(*w) >= v) break;
    __builtin_amdgcn_s_sleep(1);
  }
  asm volatile("" ::: "memory");
}

enum { EPI_QKV = 0, EPI_FFN1 = 1, EPI_RES = 2 };

template <int BN> struct BL {
  static constexpr bool PADDED = (BN == 96);
  static constexpr int RB = BN * 2;
  static constexpr int GS = PADDED ? 784 : 4 * RB;
  static constexpr int STAGE = 16 * GS;
  static constexpr int NI = PADDED ? 16 : (64 * RB / 1024);
  static DI int sw(int row) { return BN == 64 ? (((row >> 1) & 1) | (((row >> 3) & 1) << 1)) : ((row & 3) | (((row >> 3) & 1) << 2)); }
};

template <int NK, int NS, int P0, int L, int NE> struct Sched {
  static constexpr int mn(int a, int b) { return a < b ? a : b; }
  static constexpr int up(int j) { return mn(mn(P0 + 2 * j, (j < 1 ? 1 : j) + NS - 1), NK); }
  static constexpr int behind(int j) { return (up(j) - 1 - j) * L + (up(j) == NK ? NE : 0); }
};

template <int BM, int BN, int KTOT, int N, int NA, int NB, int PB, int SUB, int PD, int EPI, bool STI, bool FIRST>
DI void gemm_phase(char* smem, const u16* Ap, const u16* Bp, const u16* aux, u16* out, float* stats, int m0, int n0,
                   unsigned* wcnt, unsigned wtarget, unsigned* scnt, unsigned ph, unsigned narr, int rot0, float* csio, bool csin) {
  const int rot = ROT ? rot0 % (KTOT / (64 * SUB)) : 0;
  asm volatile("" : "+s"(Ap), "+s"(Bp), "+s"(aux), "+s"(out), "+s"(stats), "+s"(m0), "+s"(n0));
  constexpr int WM = 4, WN = 2;
  constexpr int NK = KTOT / (64 * SUB);
  constexpr int ASUB = BM * 128, BSUB = BL<BN>::STAGE;
  constexpr int ASTG = ASUB * SUB, BSTG = BSUB * SUB;
  constexpr int BOFF = NA * ASTG;
  static_assert(BOFF + NB * BSTG <= EOFF, "rings exceed LDS");
  constexpr int XCH = ((NK - 2) % NA) * ASTG;
  constexpr int MF = BM / WM / 16, NF = BN / WN / 16;
  constexpr int LA = BM / 16;
  constexpr int LB = BL<BN>::NI / 2;
  constexpr int LM = LA > LB ? LA : LB;
  constexpr bool LN = (EPI != EPI_RES);
  constexpr bool AHM = (EPI == EPI_RES && KTOT == 512);
  constexpr int NE = (EPI == EPI_RES) ? 2 : (STI ? 0 : (EPI == EPI_FFN1 ? 5 : 4));
  using SA = Sched<NK, NA, 2, LA * SUB, NE>;
  using SB = Sched<NK, NB, PB, LB * SUB, 0>;
  const unsigned lds0 = (unsigned)(size_t)(LDSP(char))smem;
  float* csb = (float*)(smem + XOFF);
  float* st = csb + 256;
  int tid = threadIdx.x;
  asm volatile("" : "+v"(tid));
  const int lane = tid & 63;
  const int wid = __builtin_amdgcn_readfirstlane(tid >> 6);
  const int l15 = lane & 15, g = lane >> 4;
  const int bq = l15 >> 2, bp = lane & 3;
  auto bfrag_off = [&](int f) -> unsigned {
    if (BL<BN>::PADDED) return (2 * g) * BL<BN>::GS + bq * BL<BN>::RB + f * 32 + bp * 8;
    const int row = 8 * g + bq;
    return row * BL<BN>::RB + ((f ^ BL<BN>::sw(row)) * 32) + bp * 8;
  };

  if (wid >= 8) {
    const int w4 = wid - 8;
    const bool isA = w4 < 2;
    const int w2 = w4 & 1;
    unsigned off[LM];
#pragma unroll
    for (int i = 0; i < LM; ++i) {
      if (isA) {
        const int ins = w2 * LA + i;
        const int row = ins * 8 + (lane >> 3), ch = (lane & 7) ^ ((row >> 1) & 7);
        off[i] = AHM ? (unsigned)((ch >> 1) * 65536 + row * 32 + (ch & 1) * 16) : (unsigned)(row * KTOT * 2 + ch * 16);
      } else {
        const int ins = w2 * LB + i;
        constexpr int CPR = BN / 8;
        if (BL<BN>::PADDED) {
          const int row = ins * 4 + lane / CPR, ch = lane % CPR;
          off[i] = (unsigned)(row * N * 2 + ch * 16);
        } else {
          const int row = ins * (64 / CPR) + lane / CPR;
          const int ch = (lane % CPR) ^ (BL<BN>::sw(row) << 1);
          off[i] = (unsigned)(row * N * 2 + ch * 16);
        }
      }
    }
    const char* abase = (const char*)Ap + (AHM ? (size_t)m0 * 32 : (size_t)m0 * KTOT * 2);
    const char* bbase = (const char*)Bp + (size_t)n0 * 2;
    auto extras = [&]() {
      if (EPI == EPI_RES) {
#pragma unroll
        for (int i = 0; i < 2; ++i) {
          const int row = (w4 * 2 + i) * 8 + (lane >> 3), ch = (lane & 7) ^ ((row >> 1) & 7);
          glds16s((const char*)aux + ((size_t)m0 * N + n0) * 2, (unsigned)(row * N * 2 + ch * 16), lds0 + EOFF + (w4 * 2 + i) * 1024);
        }
      } else if (!STI) {
#pragma unroll
        for (int i = 0; i < 4; ++i) glds16s((const char*)stats + (size_t)m0 * 128 + w4 * 256 + i * 4096, g * 1024 + l15 * 16, lds0 + EOFF + (w4 * 4 + i) * 1024);
        if (EPI == EPI_FFN1) glds16m<32>((const char*)aux + n0 * 2, (lane & 15) * 16, lds0 + EOFF + 16384);
      }
    };
    auto issueA = [&](int s) {
#pragma unroll
      for (int u = 0; u < SUB; ++u)
#pragma unroll
        for (int i = 0; i < LA; ++i)
          glds16s(abase + (size_t)(((s + rot >= NK) ? s + rot - NK : s + rot) * SUB + u) * (AHM ? 4 * 65536 : 128), off[i], lds0 + (s % NA) * ASTG + u * ASUB + (w2 * LA + i) * 1024);
      if (s == NK - 1) extras();
    };
    auto issueB = [&](int s) {
#pragma unroll
      for (int u = 0; u < SUB; ++u) {
        const unsigned slot = lds0 + BOFF + (s % NB) * BSTG + u * BSUB;
        const char* src = bbase + (size_t)(((s + rot >= NK) ? s + rot - NK : s + rot) * SUB + u) * 64 * N * 2;
#pragma unroll
        for (int i = 0; i < LB; ++i) {
          if (BL<BN>::PADDED) glds16m<BL<BN>::PADDED ? 4 * (BN / 8) : 64>(src, off[i], slot + (w2 * LB + i) * BL<BN>::GS);
          else glds16(src, off[i], slot + (w2 * LB + i) * 1024);
        }
      }
    };
    f32x2 csv = {0.f, 0.f};
    if (LN && csin && w4 == 3 && lane < BN / 2) csv = ld8s(csio + n0 + lane * 2);
    if (isA) {
      if (!FIRST) { if (LDPOLL) wait_cnt(wcnt, wtarget, lane); else lds_wait(smem, 8, ph); }
#pragma unroll
      for (int s = 0; s < SA::up(0); ++s) issueA(s);
    } else {
      if (!FIRST && !EARLYB) lds_wait(smem, 0, narr);
#pragma unroll
      for (int s = 0; s < SB::up(0); ++s) issueB(s);
    }
    const bf16x8 ONES = __builtin_bit_cast(bf16x8, u32x4{0x3f803f80u, 0x3f803f80u, 0x3f803f80u, 0x3f803f80u});
    const unsigned c_rd0 = lds0 + BOFF + bfrag_off(w4), c_rd1 = lds0 + BOFF + bfrag_off(w4 + 4);
    const unsigned a_rd = lds0 + (w4 * 32 + l15) * 128 + ((g ^ (l15 >> 1)) * 16);
    f32x4 cs0 = {0.f, 0.f, 0.f, 0.f}, cs1 = cs0, rs[2] = {cs0, cs0}, rq[2] = {cs0, cs0};
#pragma unroll
    for (int j = 0; j < NK; ++j) {
      if (isA) {
        wait_bar_n(SA::behind(j));
#pragma unroll
        for (int s = SA::up(j); s < SA::up(j + 1); ++s) issueA(s);
      } else {
        wait_bar_n(SB::behind(j) + ((j > NK - 3 + 0) && (NK - 2 >= 1) ? ((j >= NK - 1) ? NE : 0) : 0));
#pragma unroll
        for (int s = SB::up(j); s < SB::up(j + 1); ++s) issueB(s);
        if (j == NK - 2) extras();
      }
      if (LN && !csin) {
#pragma unroll
        for (int ks = 0; ks < 2; ++ks) {
          const unsigned o = (j % NB) * BSTG + ks * 8 * BL<BN>::GS;
          cs0 = __builtin_amdgcn_mfma_f32_16x16x32_bf16(CAT8(trread(c_rd0 + o), trread(c_rd0 + o + BL<BN>::GS)), ONES, cs0, 0, 0, 0);
          cs1 = __builtin_amdgcn_mfma_f32_16x16x32_bf16(CAT8(trread(c_rd1 + o), trread(c_rd1 + o + BL<BN>::GS)), ONES, cs1, 0, 0, 0);
          if (STI) {
#pragma unroll
            for (int mf = 0; mf < 2; ++mf) {
              const bf16x8 a = ldsread16((a_rd ^ (ks * 64)) + (j % NA) * ASTG + mf * 2048);
              rs[mf] = __builtin_amdgcn_mfma_f32_16x16x32_bf16(ONES, a, rs[mf], 0, 0, 0);
              rq[mf] = __builtin_amdgcn_mfma_f32_16x16x32_bf16(a, a, rq[mf], 0, 0, 0);
            }
          }
        }
        PIN(cs0); PIN(cs1);
        if (STI) { PIN(rs[0]); PIN(rs[1]); PIN(rq[0]); PIN(rq[1]); }
      }
    }
    if (LN) {
      if (csin) {
        if (w4 == 3 && lane < BN / 2) *(f32x2*)(csb + lane * 2) = csv;
      } else if (l15 == 0) {
        *(f32x4*)(csb + w4 * 16 + g * 4) = cs0;
        *(f32x4*)(csb + 64 + w4 * 16 + g * 4) = cs1;
      }
      if (STI) {
#pragma unroll
        for (int mf = 0; mf < 2; ++mf) {
          const int jj = l15 & 3;
          const float d = (jj == 0) ? rq[mf][0] : (jj == 1) ? rq[mf][1] : (jj == 2) ? rq[mf][2] : rq[mf][3];
          if (g == (l15 >> 2)) *(f32x2*)(st + (w4 * 32 + mf * 16 + l15) * 2) = f32x2{rs[mf][0], d};
        }
      } else {
        wait_vm<0>();
        f32x4 v = *(const f32x4*)(smem + EOFF + (w4 * 4) * 1024 + lane * 16);
#pragma unroll
        for (int i = 1; i < 4; ++i) v += *(const f32x4*)(smem + EOFF + (w4 * 4 + i) * 1024 + lane * 16);
#pragma unroll
        for (int i = 0; i < 4; ++i) v[i] = sum32(sum16(v[i]));
        if (g == 0) *(f32x4*)(st + (w4 * 32 + 2 * l15) * 2) = v;
      }
    }
    wait_bar<0>();
    return;
  }

  constexpr bool KSP = (EPI == EPI_RES) && KSPLIT;
  constexpr int CMF = KSP ? 2 : MF;
  const int kh = KSP ? (wid >> 2) : 0;
  const int wn = KSP ? ((wid >> 1) & 1) : wid / WM;
  const int wm = KSP ? ((wid & 1) * 2 + kh) : wid % WM;
  const int trow = m0 + wm * (MF * 16) + l15;
  const int ncol = n0 + wn * NF * 16;
  const unsigned a_rd = lds0 + ((KSP ? (wid & 1) * 32 : wm * (MF * 16)) + l15) * 128 + ((g ^ (l15 >> 1)) * 16);
  unsigned b_rd[BL<BN>::PADDED ? 1 : NF];
#pragma unroll
  for (int nf = 0; nf < (BL<BN>::PADDED ? 1 : NF); ++nf) b_rd[nf] = lds0 + BOFF + bfrag_off(wn * NF + nf) + (KSP ? kh * 8 * BL<BN>::GS : 0);
  const unsigned a_rk = KSP ? (a_rd ^ (kh * 64)) : a_rd;
  f32x4 acc[CMF][NF];
#pragma unroll
  for (int a = 0; a < CMF; ++a)
#pragma unroll
    for (int b = 0; b < NF; ++b) acc[a][b] = f32x4{0.f, 0.f, 0.f, 0.f};

  constexpr int KS = KSP ? SUB : 2 * SUB, GPS = KS / PD, NG = NK * GPS;
  bf16x8 af[2][PD][CMF], bfr[2][PD][NF];
  auto rdfrags = [&](int gi) {
    const int pb = gi & 1, s = gi / GPS;
#pragma unroll
    for (int q = 0; q < PD; ++q) {
      const int k = KSP ? 2 * ((gi % GPS) * PD + q) : (gi % GPS) * PD + q;
#pragma unroll
      for (int mf = 0; mf < CMF; ++mf) af[pb][q][mf] = ldsread16((a_rk ^ ((k & 1) * 64)) + (s % NA) * ASTG + (k >> 1) * ASUB + mf * 2048);
#pragma unroll
      for (int nf = 0; nf < NF; ++nf) {
        const unsigned o = (BL<BN>::PADDED ? b_rd[0] + nf * 32 : b_rd[nf]) + (s % NB) * BSTG + (k >> 1) * BSUB + (k & 1) * 8 * BL<BN>::GS;
        bfr[pb][q][nf] = CAT8(trread(o), trread(o + BL<BN>::GS));
      }
    }
  };
  if (!FIRST && !LDPOLL && wid < NPOLL) {
    if (wid) __builtin_amdgcn_s_sleep(6);
    wait_cnt(wcnt, wtarget, lane); acquire_cu(); *(volatile unsigned*)(smem + SYNC + 8) = ph;
  }
  lds_bar();
  rdfrags(0);
#pragma unroll
  for (int gi = 0; gi < NG; ++gi) {
    if (gi + 1 < NG) {
      if ((gi + 1) % GPS == 0) lds_bar();
      SBAR();
      rdfrags(gi + 1);
    }
    const int pb = gi & 1;
#pragma unroll
    for (int q = 0; q < PD; ++q)
#pragma unroll
      for (int nf = 0; nf < NF; ++nf)
#pragma unroll
        for (int mf = 0; mf < CMF; ++mf)
          acc[mf][nf] = __builtin_amdgcn_mfma_f32_16x16x32_bf16(bfr[pb][q][nf], af[pb][q][mf], acc[mf][nf], 0, 0, 0);
    if (gi + 1 < NG) {
      constexpr int NM = PD * CMF * NF, ND = PD * (CMF + 2 * NF);
      constexpr int PER = (ND + NM - 1) / NM;
#pragma unroll
      for (int i = 0; i < NM; ++i) {
        __builtin_amdgcn_sched_group_barrier(0x100, PER, 0);
        __builtin_amdgcn_sched_group_barrier(0x8, 1, 0);
      }
    }
#pragma unroll
    for (int mf = 0; mf < CMF; ++mf)
#pragma unroll
      for (int nf = 0; nf < NF; ++nf) PIN(acc[mf][nf]);
  }
  if (KSP) {
#pragma unroll
    for (int nf = 0; nf < NF; ++nf) *(f32x4*)(smem + XCH + (wid * NF + nf) * 1024 + lane * 16) = kh ? acc[0][nf] : acc[CMF - 1][nf];
  }
  lds_bar();
  if (KSP) {
#pragma unroll
    for (int nf = 0; nf < NF; ++nf) acc[0][nf] = (kh ? acc[CMF - 1][nf] : acc[0][nf]) + *(const f32x4*)(smem + XCH + ((wid ^ 4) * NF + nf) * 1024 + lane * 16);
  }
  if (LN) {
    f32x4 c0[NF];
    float rstd[MF], nmr[MF];
#pragma unroll
    for (int nf = 0; nf < NF; ++nf) c0[nf] = *(const f32x4*)(csb + (wn * NF + nf) * 16 + g * 4);
    if (CSREUSE && !csin && m0 == 0 && wm == 0 && l15 == 0) {
#pragma unroll
      for (int nf = 0; nf < NF; ++nf) st16(csio + ncol + nf * 16 + g * 4, __builtin_bit_cast(u32x4, c0[nf]));
    }
#pragma unroll
    for (int mf = 0; mf < MF; ++mf) {
      const f32x2 a = *(const f32x2*)(st + (wm * (MF * 16) + mf * 16 + l15) * 2);
      const float mean = a[0] * (1.f / KTOT);
      const float var = fmaxf(a[1] * (1.f / KTOT) - mean * mean, 0.f);
      rstd[mf] = __builtin_amdgcn_rsqf(var + 1e-5f);
      nmr[mf] = -mean * rstd[mf];
    }
    if (EPI == EPI_QKV) {
      const int tk = trow + (g & 1) * 16;
      const unsigned lin = tk * 16 + (g >> 1) * 8;
      const unsigned kof = (tk >> 5) * 512 + (g >> 1) * 256 + (tk & 31) * 8;
#pragma unroll
      for (int nf = 0; nf < NF; ++nf) {
        const int hh = (ncol >> 4) + nf;
        const bool isq = hh < 32, isk = (hh >> 5) == 1;
        const float sc = isq ? 0.25f * 1.4426950408889634f : 1.f;
        u16* base = out + (size_t)hh * (SEQ * 16);
        u32x2 o[MF];
#pragma unroll
        for (int mf = 0; mf < MF; ++mf) {
          const f32x4 y = acc[mf][nf] * (rstd[mf] * sc) + c0[nf] * (nmr[mf] * sc);
          o[mf] = u32x2{cvtpk(y[0], y[1]), cvtpk(y[2], y[3])};
        }
        st16(base + (isk ? kof : lin), pair16(o[0], o[MF - 1]));
      }
    } else {
      u32x2 o[MF][NF];
#pragma unroll
      for (int nf = 0; nf < NF; ++nf) {
        const u32x2 bb = *(const u32x2*)(smem + EOFF + 16384 + ((wn * NF + nf) * 16 + g * 4) * 2);
        const f32x4 bias = {bflo(bb[0]), bfhi(bb[0]), bflo(bb[1]), bfhi(bb[1])};
#pragma unroll
        for (int mf = 0; mf < MF; ++mf) {
          const f32x4 h = acc[mf][nf] * rstd[mf] + (c0[nf] * nmr[mf] + bias);
          const f32x4 t = h * 1.4426950408889634f;
          f32x4 u, rc;
#pragma unroll
          for (int j = 0; j < 4; ++j) u[j] = __builtin_amdgcn_exp2f(t[j]);
          const f32x4 d = u * (u * -0.5f - 1.f) - 1.f;
#pragma unroll
          for (int j = 0; j < 4; ++j) rc[j] = __builtin_amdgcn_rcpf(d[j]);
          const f32x4 a = h * rc + h;
          o[mf][nf] = u32x2{cvtpk(a[0], a[1]), cvtpk(a[2], a[3])};
          if (nf & 1) st16pair(out + (size_t)(trow + mf * 16) * N + ncol + (nf - 1) * 16, g, o[mf][nf - 1], o[mf][nf]);
        }
      }
    }
  } else {
#pragma unroll
    for (int mf = 0; mf < MF; ++mf) {
      const int tok = trow + mf * 16;
      float s1 = 0.f, s2 = 0.f;
      u32x2 o[NF];
#pragma unroll
      for (int nf = 0; nf < NF; ++nf) {
        const u32x2 rr = *(const u32x2*)(smem + EOFF + (wm * (MF * 16) + mf * 16 + l15) * 128 + ((((wn * NF + nf) * 2 + (g >> 1)) ^ (l15 >> 1)) * 16) + (g & 1) * 8);
        const f32x4 r = acc[mf][nf];
        o[nf] = u32x2{cvtpk(r[0] + bflo(rr[0]), r[1] + bfhi(rr[0])), cvtpk(r[2] + bflo(rr[1]), r[3] + bfhi(rr[1]))};
        const float e0 = bflo(o[nf][0]), e1 = bfhi(o[nf][0]), e2 = bflo(o[nf][1]), e3 = bfhi(o[nf][1]);
        s1 += (e0 + e1) + (e2 + e3);
        s2 += (e0 * e0 + e1 * e1) + (e2 * e2 + e3 * e3);
        if (nf & 1) st16pair(out + (size_t)tok * N + ncol + (nf - 1) * 16, g, o[nf - 1], o[nf]);
      }
      s1 = sum32(sum16(s1)); s2 = sum32(sum16(s2));
      if (g == 0) st8(stats + (tok >> 7) * 4096 + (ncol >> 5) * 256 + (tok & 127) * 2, f32x2{s1, s2});
    }
  }
  if (!FIRST) signal_done(smem, scnt, lane, EPI == EPI_QKV ? 48 : 16, EPI == EPI_QKV ? (m0 >> 7) : ((blockIdx.x >> 3) & 15));
}

constexpr int AT_K = 0, AT_V = 65536, AT_ONE = 131072, AT_Q = AT_ONE + 6144, AT_PF = AT_Q + 8 * 9 * 256;
static_assert(AT_PF + 1024 <= XOFF, "attention LDS");

#define ATT_HALF(SC, OO, VC, MID)                                                                            \
  {                                                                                                          \
    _Pragma("unroll") for (int i = 0; i < 4; ++i) SC[i] = __builtin_amdgcn_exp2f(SC[i]);                     \
    SBAR(); MID; SBAR();                                                                                     \
    _Pragma("unroll") for (int i = 4; i < 9; ++i) SC[i] = __builtin_amdgcn_exp2f(SC[i]);                     \
    u32x4 p0 = {cvtpk(SC[0], SC[1]), cvtpk(SC[2], SC[3]), cvtpk(SC[4], SC[5]), cvtpk(SC[6], SC[7])};         \
    PIN(p0); SBAR();                                                                                         \
    SC[9] = __builtin_amdgcn_exp2f(SC[9]); SBAR();                                                           \
    OO = MFMA32(CAT8(VC[0], VC[1]), __builtin_bit_cast(bf16x8, p0), OO); SBAR();                             \
    _Pragma("unroll") for (int i = 10; i < 16; ++i) SC[i] = __builtin_amdgcn_exp2f(SC[i]);                   \
    u32x4 p1 = {cvtpk(SC[8], SC[9]), cvtpk(SC[10], SC[11]), cvtpk(SC[12], SC[13]), cvtpk(SC[14], SC[15])};   \
    OO = MFMA32(CAT8(VC[2], VC[3]), __builtin_bit_cast(bf16x8, p1), OO); SBAR();                             \
  }
#define ATT_TILE(VC, VN, T)                                                                                  \
  ATT_HALF(s0, o0, VC, { s1 = MFMA32(kf, qf1, cm1); PIN(s1); kf = ldsread16(kaddr + ((T) + 1) * 1024);       \
       VN[0] = trread(vaddr + ((T) + 1) * 1024); VN[1] = trread(vaddr + ((T) + 1) * 1024 + 256); })          \
  ATT_HALF(s1, o1, VC, { s0 = MFMA32(kf, qf0, cm0); PIN(s0);                                                 \
       VN[2] = trread(vaddr + ((T) + 1) * 1024 + 512); VN[3] = trread(vaddr + ((T) + 1) * 1024 + 768); })

template <bool FAST>
DI void attn_pass(unsigned lds0, int lane, int ks, const bf16x8 qf0, const bf16x8 qf1, float nm0, float nm1, f32x16& o0, f32x16& o1) {
  const int h = lane >> 5;
  unsigned kaddr = lds0 + AT_K + ks * 32768 + h * 512 + (lane & 31) * 16;
  const bool cl = (lane >> 4) & 1;
  const int qq = (lane & 15) >> 2, pp = lane & 3;
  unsigned vaddr = cl ? lds0 + AT_ONE + (4 * (h ^ 1) + qq) * 32 + pp * 8 : lds0 + AT_V + ks * 32768 + (4 * h + qq) * 32 + pp * 8;
  const unsigned vstep = cl ? 0u : 4096u;
  f32x16 cm0, cm1, s0, s1;
  s16x4 vA[4], vB[4];
#pragma unroll
  for (int i = 0; i < 16; ++i) { cm0[i] = FAST ? 0.f : nm0; cm1[i] = FAST ? 0.f : nm1; o0[i] = 0.f; o1[i] = 0.f; }
  bf16x8 kf = ldsread16(kaddr);
  s0 = MFMA32(kf, qf0, cm0);
  vA[0] = trread(vaddr); vA[1] = trread(vaddr + 256); vA[2] = trread(vaddr + 512); vA[3] = trread(vaddr + 768);
  for (int it = 0; it < 8; ++it) {
    PIN(kaddr); PIN(vaddr);
    ATT_TILE(vA, vB, 0) ATT_TILE(vB, vA, 1) ATT_TILE(vA, vB, 2) ATT_TILE(vB, vA, 3)
    kaddr += 4096; vaddr += vstep;
    if (ATBAR || it == 0) lds_bar();
  }
}

DI void attn_phase(char* smem, const u16* QKV, u16* O, const u16* wo, const u16* w1, const u16* w2, unsigned* cq, bool wait, unsigned* scnt) {
  asm volatile("" : "+s"(QKV), "+s"(O));
  const unsigned lds0 = (unsigned)(size_t)(LDSP(char))smem;
  int tid = threadIdx.x;
  asm volatile("" : "+v"(tid));
  const int lane = tid & 63;
  const int wid = __builtin_amdgcn_readfirstlane(tid >> 6);
  const int bid = blockIdx.x;
  const int head = (bid & 7) * 4 + ((bid >> 3) & 3), qb = bid >> 5;
  volatile unsigned* flag = (volatile unsigned*)(smem + SYNC + 4);
  if (wid >= 8) {
    const int w4 = wid - 8;
    const unsigned l16 = lane * 16;
#pragma unroll
    for (int i = 0; i < 2; ++i) { const int t = (w4 * 2 + i) * 64 + lane; if (t < 384) *(u32x4*)(smem + AT_ONE + t * 16) = u32x4{(t & 1) ? 0u : 0x3f80u, 0u, 0u, 0u}; }
    if (tid == 767) *flag = 0u;
    if (wait) lds_wait(smem, 8, 5u);
    const char* qsrc = (const char*)QKV + ((size_t)head * SEQ + qb * 256) * 32 + w4 * 2048;
    const char* src = (const char*)QKV + (size_t)(32 + 32 * (w4 >> 1) + head) * 65536 + (w4 & 1) * 32768;
    const unsigned dst = lds0 + (w4 >> 1) * 65536 + (w4 & 1) * 32768;
    glds16s(qsrc, l16, lds0 + AT_Q + w4 * 2048); glds16s(qsrc + 1024, l16, lds0 + AT_Q + w4 * 2048 + 1024);
#pragma unroll
    for (int i = 0; i < 8; ++i) glds16s(src + i * 1024, l16, dst + i * 1024);
    wait_bar<0>();
#pragma unroll
    for (int i = 8; i < 32; ++i) glds16s(src + i * 1024, l16, dst + i * 1024);
    wait_bar<0>();
    {
      const int u = (bid >> 3) * 4 + w4;
      const char* p1 = (u < 28) ? (const char*)wo + u * 18432 : (const char*)w1 + (u - 28) * 18432;
      const int n1 = (u < 28) ? (u == 27 ? 26624 : 18432) : (u == 127 ? 10240 : 18432);
      const char* p2 = (const char*)w2 + (size_t)(bid * 4 + w4) * 1792;
      int off = 0;
      for (int it = 1; it < 8; ++it) {
#pragma unroll
        for (int i = 0; i < 4; ++i) { if (off < n1) glds16(p1 + off, l16, lds0 + AT_PF); off += 1024; }
        if (it == 1) { glds16(p2, l16, lds0 + AT_PF); glds16m<48>(p2 + 1024, l16, lds0 + AT_PF); }
        if (ATBAR) { if (it == 7) wait_bar<0>(); else lds_bar(); } else __builtin_amdgcn_s_sleep(40);
      }
      wait_vm<0>();
    }
    lds_bar();
    if (__builtin_amdgcn_readfirstlane(*flag) != 0u) {
      for (int it = 0; it < (ATBAR ? 9 : 2); ++it) lds_bar();
    }
    return;
  }
  const int wq = wid & 3, ks = wid >> 2;
  if (wait && wid == 0) {
    const int hh = (lane < 3 ? lane : 0) * 32 + head;
    gu32* c = (gu32*)(cq + ((hh / 6) * 64 + (hh % 6) * 8 + qb) * 32);
    for (unsigned spins = 0; spins < 200000u; ++spins) {
      unsigned v = 16;
      if (lane < 3) v = __hip_atomic_load(c, __ATOMIC_RELAXED, __HIP_MEMORY_SCOPE_AGENT);
      if (__all(v >= 16u)) break;
    }
    asm volatile("" ::: "memory");
    *(volatile unsigned*)(smem + SYNC + 8) = 5u;
  }
  lds_bar();
  const bf16x8 qf0 = ldsread16(lds0 + AT_Q + wq * 2048 + (lane & 31) * 32 + (lane >> 5) * 16);
  const bf16x8 qf1 = ldsread16(lds0 + AT_Q + wq * 2048 + 1024 + (lane & 31) * 32 + (lane >> 5) * 16);
  f32x16 o0, o1;
  attn_pass<true>(lds0, lane, ks, qf0, qf1, 0.f, 0.f, o0, o1);
  float* xb = (float*)(smem + AT_Q);
  if (lane < 32 && !(o0[8] >= 8.67e-19f && o0[8] <= 1.26e30f && o1[8] >= 8.67e-19f && o1[8] <= 1.26e30f)) *flag = 1u;
#pragma unroll
  for (int i = 0; i < 9; ++i) xb[(wid * 9 + i) * 64 + lane] = ks ? o0[i] : o1[i];
  lds_bar();
  if (__builtin_amdgcn_readfirstlane(*flag) != 0u) {
    float m0 = -3.0e38f, m1 = -3.0e38f;
    const unsigned kaddr = lds0 + AT_K + (lane >> 5) * 512 + (lane & 31) * 16;
    f32x16 z;
#pragma unroll
    for (int i = 0; i < 16; ++i) z[i] = 0.f;
    for (int t = 0; t < 64; ++t) {
      const bf16x8 kf = ldsread16(kaddr + t * 1024);
      const f32x16 sa = MFMA32(kf, qf0, z), sb = MFMA32(kf, qf1, z);
#pragma unroll
      for (int i = 0; i < 16; ++i) { m0 = fmaxf(m0, sa[i]); m1 = fmaxf(m1, sb[i]); }
    }
    m0 = max32(m0); m1 = max32(m1);
    attn_pass<false>(lds0, lane, ks, qf0, qf1, -m0, -m1, o0, o1);
#pragma unroll
    for (int i = 0; i < 9; ++i) xb[(wid * 9 + i) * 64 + lane] = ks ? o0[i] : o1[i];
    lds_bar();
  }
  f32x16 o = ks ? o1 : o0;
#pragma unroll
  for (int i = 0; i < 9; ++i) o[i] += xb[((wid ^ 4) * 9 + i) * 64 + lane];
  auto lr = __builtin_amdgcn_permlane32_swap(__float_as_uint(o[8]), __float_as_uint(o[8]), false, false);
  const float rl = __builtin_amdgcn_rcpf(__uint_as_float(lr[0]));
  unsigned x0 = cvtpk(o[0] * rl, o[1] * rl), x1 = cvtpk(o[2] * rl, o[3] * rl);
  unsigned y0 = cvtpk(o[4] * rl, o[5] * rl), y1 = cvtpk(o[6] * rl, o[7] * rl);
  auto r0 = __builtin_amdgcn_permlane32_swap(x0, y0, false, false);
  auto r1 = __builtin_amdgcn_permlane32_swap(x1, y1, false, false);
  const int q0 = qb * 256 + wq * 64 + ks * 32;
  st16(O + ((size_t)head * SEQ + q0 + (lane & 31)) * 16 + (lane >> 5) * 8, u32x4{r0[0], r1[0], r0[1], r1[1]});
  signal_done(smem, scnt, lane, 32, bid & 31);
}

constexpr size_t MB = 1 << 20;
constexpr size_t WS_QKV = 0, WS_O = 12 * MB, WS_X1 = 16 * MB, WS_X2 = 20 * MB, WS_H = 22 * MB, WS_ST = 38 * MB, WS_CNT = 39 * MB, WS_CS = 40 * MB;
constexpr int C_A = 0, C_1 = 256, C_2 = 512, C_3 = 768, C_LAYER = 1024, C_Q = 2048, C_TOTAL = 3072;

struct Params {
  const u16 *x, *wqkv, *wo, *w1, *b1, *w2;
  u16* out;
  char* ws;
};

extern "C" __global__ __launch_bounds__(768, 3) void k_mega(Params p) {
  __shared__ __attribute__((aligned(256))) char smem[LDS_BYTES];
  if (threadIdx.x == 0) { *(unsigned*)(smem + SYNC) = 0u; *(unsigned*)(smem + SYNC + 8) = 0u; }
  if (__builtin_amdgcn_readfirstlane(threadIdx.x) >= 512) __builtin_amdgcn_s_setprio(3);
  const int bid = blockIdx.x, idx = bid >> 3;
  const int team = (bid & 7) * 2 + (idx >> 4), mem = idx & 15;
  unsigned narr = 0;
  for (int layer = 0; layer < 2; ++layer) {
    asm volatile("" : "+s"(p.ws));
    unsigned* cnt = (unsigned*)(p.ws + WS_CNT);
    unsigned* cl = cnt + layer * C_LAYER * 32;
    u16* O = (u16*)(p.ws + WS_O + layer * 2 * MB);
    u16* X1 = (u16*)(p.ws + WS_X1 + layer * 2 * MB);
    u16* X2 = (u16*)(p.ws + WS_X2);
    u16* H = (u16*)(p.ws + WS_H + layer * 8 * MB);
    float* st1 = (float*)(p.ws + WS_ST + layer * 2 * 262144);
    float* st2 = st1 + 65536;
    attn_phase(smem, (const u16*)(p.ws + WS_QKV + layer * 6 * MB), O, p.wo, p.w1, p.w2, cnt + C_Q * 32, layer != 0, cl + (C_A + (bid >> 5) * 32) * 32);
    narr += 8;
    gemm_phase<64, 64, 512, 512, CFG_O, EPI_RES, true, false>(smem, O, p.wo, layer ? X2 : p.x, X1, st1, team * 128 + (mem >> 3) * 64, (mem & 7) * 64,
                                                                    cl + (C_A + team * 16 + mem) * 32, 32, cl + (C_1 + team * 16) * 32, 1 + layer * 8, narr, mem + team, nullptr, false);
    narr += 8;
    if (mem < 14) {
      gemm_phase<128, 128, 512, 1792, CFG_1, EPI_FFN1, false, false>(smem, X1, p.w1, p.b1, H, st1, team * 128, mem * 128,
                                                                          cl + (C_1 + team * 16 + mem) * 32, 16, cl + (C_2 + team * 16) * 32, 2 + layer * 8, narr, mem + team * 4, (float*)(p.ws + WS_CS) + 2048, CSREUSE && layer != 0);
      narr += 8;
    } else if (W2WARM) {
      int tid = threadIdx.x;
      asm volatile("" : "+v"(tid));
      const int w = __builtin_amdgcn_readfirstlane(tid >> 6);
      const char* src = (const char*)p.w2 + (size_t)((team & 1) * 2 + (mem - 14)) * 458752;
      const unsigned lds0 = (unsigned)(size_t)(LDSP(char))smem;
      for (int i = w; i < 448; i += 12) { glds16(src + (size_t)i * 1024, (tid & 63) * 16, lds0 + w * 1024); wait_vm<8>(); }
      wait_bar<0>();
    }
    gemm_phase<64, 64, 1792, 512, CFG_2, EPI_RES, true, false>(smem, H, p.w2, X1, layer ? p.out : X2, st2, team * 128 + (mem >> 3) * 64, (mem & 7) * 64,
                                                                     cl + (C_2 + team * 16 + mem) * 32, 14, cl + (C_3 + team * 16) * 32, 3 + layer * 8, narr, (mem & 7) * 2 + (mem >> 3) + (team & 1) * 7, nullptr, false);
    narr += 8;
    if (layer == 0) {
      gemm_phase<128, 96, 512, 1536, CFG_Q, EPI_QKV, false, false>(smem, X2, p.wqkv, nullptr, (u16*)(p.ws + WS_QKV + 6 * MB), st2, team * 128, mem * 96,
                                                                        cl + (C_3 + team * 16 + mem) * 32, 16, cnt + (C_Q + mem * 64) * 32, 4, narr, mem + team * 4, (float*)(p.ws + WS_CS), CSREUSE);
      narr += 8;
    }
  }
}
extern "C" __global__ void k_zend() {}

extern "C" __global__ __launch_bounds__(768, 3) void k_qkv1(Params p) {
  __shared__ __attribute__((aligned(256))) char smem[LDS_BYTES];
  const int bid = blockIdx.x, xcd = bid & 7, idx = bid >> 3;
  if (__builtin_amdgcn_readfirstlane(threadIdx.x) >= 512) __builtin_amdgcn_s_setprio(3);
  if (threadIdx.x < C_TOTAL / 32) *(u32x4*)(p.ws + WS_CNT + (bid * (C_TOTAL / 32) + threadIdx.x) * 16) = u32x4{0u, 0u, 0u, 0u};
  unsigned codev = 0;
  {
    unsigned long long lo, hi;
    asm volatile("s_getpc_b64 s[20:21]\n\ts_add_u32 s20, s20, k_mega@rel32@lo+4\n\ts_addc_u32 s21, s21, k_mega@rel32@hi+12\n\ts_mov_b64 %0, s[20:21]\n\t"
                 "s_getpc_b64 s[20:21]\n\ts_add_u32 s20, s20, k_zend@rel32@lo+4\n\ts_addc_u32 s21, s21, k_zend@rel32@hi+12\n\ts_mov_b64 %1, s[20:21]"
                 : "=s"(lo), "=s"(hi)::"s20", "s21", "scc");
    const unsigned long long a = lo + (unsigned long long)(idx * 2048 + threadIdx.x * 4);
    if (threadIdx.x < 512 && a + 4 <= hi) codev = *(const __attribute__((address_space(1))) unsigned*)a;
  }
  unsigned touch = 0;
  if (TLBW && threadIdx.x >= 64 && threadIdx.x < 128) {
    const unsigned i = (threadIdx.x - 64) + 64 * (idx & 15);
    if (i < 656) touch = *(const unsigned*)(p.ws + (size_t)i * 65536);
    else if (i < 688) touch = *(const unsigned*)((const char*)p.out + (size_t)(i - 656) * 65536);
  }
  if (TOUCH && threadIdx.x < 512) {
    const unsigned t = bid * 512 + threadIdx.x;
    if (t < 32768) touch = *(const unsigned*)((const char*)p.x + (size_t)t * 64);
    else if (t < 57344) touch = *(const unsigned*)((const char*)p.wqkv + (size_t)(t - 32768) * 64);
  }
  gemm_phase<128, 96, 512, 1536, CFG_F, EPI_QKV, true, true>(smem, p.x, p.wqkv, nullptr, (u16*)(p.ws + WS_QKV), nullptr,
                                                                   ((xcd >> 1) * 4 + idx / 8) * 128, ((xcd & 1) * 8 + idx % 8) * 96, nullptr, 0, nullptr, 0, 0, idx + (idx >> 3), (float*)(p.ws + WS_CS), false);
  asm volatile("" ::"v"(codev), "v"(touch));
}

extern "C" void kernel_launch(void* const* d_in, const int* in_sizes, int n_in, void* d_out, int out_size, void* d_ws,
                              size_t ws_size, hipStream_t stream) {
  Params p;
  p.x = (const u16*)d_in[0]; p.wqkv = (const u16*)d_in[1]; p.wo = (const u16*)d_in[2];
  p.w1 = (const u16*)d_in[4]; p.b1 = (const u16*)d_in[5]; p.w2 = (const u16*)d_in[6];
  p.out = (u16*)d_out; p.ws = (char*)d_ws;
  k_qkv1<<<256, 768, 0, stream>>>(p);
  k_mega<<<256, 768, 0, stream>>>(p);
}
```

```cpp
#include <hip/hip_runtime.h>
#include <hip/hip_bf16.h>
#include <stdint.h>

typedef short bf16x8 __attribute__((ext_vector_type(8)));
typedef short s16x4 __attribute__((ext_vector_type(4)));
typedef __bf16 bf16x2 __attribute__((ext_vector_type(2)));
typedef float f32x2 __attribute__((ext_vector_type(2)));
typedef float f32x4 __attribute__((ext_vector_type(4)));
typedef float f32x16 __attribute__((ext_vector_type(16)));
typedef unsigned u32x2 __attribute__((ext_vector_type(2)));
typedef unsigned u32x4 __attribute__((ext_vector_type(4)));
typedef unsigned short u16;
typedef __attribute__((address_space(1))) unsigned gu32;
#define LDSP(T) __attribute__((address_space(3))) T*
#define DI __device__ __forceinline__
#define SBAR() __builtin_amdgcn_sched_barrier(0)
#define PIN(x) asm volatile("" : "+v"(x))
#define MFMA16(a, b, c) __builtin_amdgcn_mfma_f32_16x16x32_bf16(a, b, c, 0, 0, 0)
#define MFMA32(a, b, c) __builtin_amdgcn_mfma_f32_32x32x16_bf16(a, b, c, 0, 0, 0)
#define CAT8(a, b) __builtin_shufflevector(a, b, 0, 1, 2, 3, 4, 5, 6, 7)

#define F1BN 128

constexpr int SEQ = 2048, DM = 512, FF = 1792;

DI unsigned cvtpk(float lo, float hi) { return __builtin_bit_cast(unsigned, __builtin_convertvector(f32x2{lo, hi}, bf16x2)); }
DI float bflo(unsigned u) { return __uint_as_float(u << 16); }
DI float bfhi(unsigned u) { return __uint_as_float(u & 0xffff0000u); }

DI void glds16(const void* sbase, unsigned voff, unsigned lds_addr) {
  unsigned sv;
  asm volatile("s_mov_b32 %0, m0\n\ts_mov_b32 m0, %3\n\ts_nop 0\n\tglobal_load_lds_dwordx4 %1, %2\n\ts_mov_b32 m0, %0"
               : "=&s"(sv) : "v"(voff), "s"(sbase), "s"(lds_addr) : "memory");
}
DI void glds16s(const void* sbase, unsigned voff, unsigned lds_addr) {
  unsigned sv;
  asm volatile("s_mov_b32 %0, m0\n\ts_mov_b32 m0, %3\n\ts_nop 0\n\tglobal_load_lds_dwordx4 %1, %2 sc1\n\ts_mov_b32 m0, %0"
               : "=&s"(sv) : "v"(voff), "s"(sbase), "s"(lds_addr) : "memory");
}
template <int NL> DI void glds16m(const void* sbase, unsigned voff, unsigned lds_addr) {
  unsigned sv;
  asm volatile("s_mov_b32 %0, m0\n\ts_mov_b32 m0, %3\n\ts_mov_b32 exec_hi, %4\n\tglobal_load_lds_dwordx4 %1, %2\n\ts_mov_b32 exec_hi, -1\n\ts_mov_b32 m0, %0"
               : "=&s"(sv) : "v"(voff), "s"(sbase), "s"(lds_addr), "i"((unsigned)((1ull << (NL - 32)) - 1)) : "memory");
}
template <int N> DI void wait_bar() { asm volatile("s_waitcnt vmcnt(%0) lgkmcnt(0)\n\ts_barrier" ::"i"(N) : "memory"); }
DI void wait_bar_n(int n) { asm volatile("s_waitcnt vmcnt(%0) lgkmcnt(0)\n\ts_barrier" ::"i"(n) : "memory"); }
template <int N> DI void wait_vm() { asm volatile("s_waitcnt vmcnt(%0)" ::"i"(N) : "memory"); }
DI void lds_bar() { asm volatile("s_waitcnt lgkmcnt(0)\n\ts_barrier" ::: "memory"); }
DI void st16(void* ptr, u32x4 v) { asm volatile("global_store_dwordx4 %0, %1, off sc1\n\ts_nop 1" ::"v"(ptr), "v"(v) : "memory"); }
DI void st8(void* ptr, f32x2 v) { asm volatile("global_store_dwordx2 %0, %1, off sc1" ::"v"(ptr), "v"(v) : "memory"); }
DI f32x2 ld8s(const void* p) {
  return __builtin_bit_cast(f32x2, __hip_atomic_load((const __attribute__((address_space(1))) unsigned long long*)p, __ATOMIC_RELAXED, __HIP_MEMORY_SCOPE_AGENT));
}
DI u32x4 pair16(u32x2 a, u32x2 b) {
  auto r0 = __builtin_amdgcn_permlane16_swap(a[0], b[0], false, false);
  auto r1 = __builtin_amdgcn_permlane16_swap(a[1], b[1], false, false);
  return u32x4{r0[0], r1[0], r0[1], r1[1]};
}
DI void st16pair(u16* rowp, int g, u32x2 a, u32x2 b) { st16(rowp + (g & 1) * 16 + (g >> 1) * 8, pair16(a, b)); }
DI s16x4 trread(unsigned addr) { return __builtin_bit_cast(s16x4, __builtin_amdgcn_ds_read_tr16_b64_v4i16((LDSP(s16x4))(size_t)addr)); }
DI bf16x8 ldsread16(unsigned addr) { return *(const LDSP(bf16x8))(size_t)addr; }
DI float sum16(float v) { auto r = __builtin_amdgcn_permlane16_swap(__float_as_uint(v), __float_as_uint(v), false, false); return __uint_as_float(r[0]) + __uint_as_float(r[1]); }
DI float sum32(float v) { auto r = __builtin_amdgcn_permlane32_swap(__float_as_uint(v), __float_as_uint(v), false, false); return __uint_as_float(r[0]) + __uint_as_float(r[1]); }
DI float max32(float v) { auto r = __builtin_amdgcn_permlane32_swap(__float_as_uint(v), __float_as_uint(v), false, false); return fmaxf(__uint_as_float(r[0]), __uint_as_float(r[1])); }

constexpr int EOFF = 143360;
constexpr int XOFF = EOFF + 17408;
constexpr int SYNC = XOFF + 2048;
constexpr int LDS_BYTES = SYNC + 256;
#define SW(off) ((volatile LDSP(unsigned))((LDSP(char))smem + SYNC + (off)))

DI void signal_done(char* smem, unsigned* cnt, int lane, int nrep) {
  wait_vm<0>();
  unsigned old = 0;
  if (lane == 0) old = __hip_atomic_fetch_add((LDSP(unsigned))((LDSP(char))smem + SYNC), 1u, __ATOMIC_RELAXED, __HIP_MEMORY_SCOPE_WORKGROUP);
  if ((__builtin_amdgcn_readfirstlane(old) & 7u) == 7u && lane < nrep)
    __hip_atomic_fetch_add((gu32*)(cnt + lane * 32), 1u, __ATOMIC_RELAXED, __HIP_MEMORY_SCOPE_AGENT);
}
DI void wait_cnt(unsigned* c, unsigned target, int lane, int nc) {
  for (unsigned spins = 0; spins < 200000u; ++spins) {
    unsigned v = target;
    if (lane < nc) v = __hip_atomic_load((gu32*)c, __ATOMIC_RELAXED, __HIP_MEMORY_SCOPE_AGENT);
    if (__all(v >= target)) break;
  }
  asm volatile("" ::: "memory");
}
DI void lds_wait(char* smem, int off, unsigned v) {
  for (unsigned spins = 0; spins < 8000000u; ++spins) {
    if ((unsigned)__builtin_amdgcn_readfirstlane(*SW(off)) >= v) break;
    __builtin_amdgcn_s_sleep(1);
  }
  asm volatile("" ::: "memory");
}

enum { EPI_QKV = 0, EPI_FFN1 = 1, EPI_RES = 2 };

template <int BN> struct BL {
  static constexpr bool PADDED = (BN == 96 || BN == 112);
  static constexpr int RB = BN * 2;
  static constexpr int GS = BN == 96 ? 784 : BN == 112 ? 960 : 4 * RB;
  static constexpr int STAGE = 16 * GS;
  static constexpr int NI = PADDED ? 16 : (64 * RB / 1024);
  static DI int sw(int row) { return BN == 64 ? (((row >> 1) & 1) | (((row >> 3) & 1) << 1)) : ((row & 3) | (((row >> 3) & 1) << 2)); }
  static DI unsigned frag(int f, int lane) {
    const int g = lane >> 4, bq = (lane & 15) >> 2, bp = lane & 3, row = 8 * g + bq;
    return PADDED ? (2 * g) * GS + bq * RB + f * 32 + bp * 8 : row * RB + ((f ^ sw(row)) * 32) + bp * 8;
  }
};

template <int NK, int NS, int P0, int L, int NE> struct Sched {
  static constexpr int mn(int a, int b) { return a < b ? a : b; }
  static constexpr int up(int j) { return mn(mn(P0 + 2 * j, (j < 1 ? 1 : j) + NS - 1), NK); }
  static constexpr int behind(int j) { return (up(j) - 1 - j) * L + (up(j) == NK ? NE : 0); }
};

template <int BM, int BN, int KTOT, int N, int NA, int NB, int SUB, int PD, int EPI, bool FIRST, int NF>
DI void gemm_compute(char* smem, u16* out, float* stats, int m0, int n0, int wm, int f0, int lane, unsigned* scnt, float* csio, bool csin) {
  constexpr int NK = KTOT / (64 * SUB);
  constexpr int ASUB = BM * 128, BSUB = BL<BN>::STAGE, ASTG = ASUB * SUB, BSTG = BSUB * SUB, BOFF = NA * ASTG;
  constexpr int MF = BM / 64;
  constexpr bool LN = (EPI != EPI_RES);
  const unsigned lds0 = (unsigned)(size_t)(LDSP(char))smem;
  const float* csb = (const float*)(smem + XOFF);
  const float* st = csb + 256;
  const int l15 = lane & 15, g = lane >> 4;
  const int trow = m0 + wm * (MF * 16) + l15;
  const int ncol = n0 + f0 * 16;
  const unsigned a_rd = lds0 + (wm * (MF * 16) + l15) * 128 + ((g ^ (l15 >> 1)) * 16);
  unsigned b_rd[BL<BN>::PADDED ? 1 : NF];
#pragma unroll
  for (int nf = 0; nf < (BL<BN>::PADDED ? 1 : NF); ++nf) b_rd[nf] = lds0 + BOFF + BL<BN>::frag(f0 + nf, lane);
  f32x4 acc[MF][NF];
#pragma unroll
  for (int a = 0; a < MF; ++a)
#pragma unroll
    for (int b = 0; b < NF; ++b) acc[a][b] = f32x4{0.f, 0.f, 0.f, 0.f};
  constexpr int KS = 2 * SUB, GPS = KS / PD, NG = NK * GPS;
  bf16x8 af[2][PD][MF], bfr[2][PD][NF];
  auto rdfrags = [&](int gi) {
    const int pb = gi & 1, s = gi / GPS;
#pragma unroll
    for (int q = 0; q < PD; ++q) {
      const int k = (gi % GPS) * PD + q;
#pragma unroll
      for (int mf = 0; mf < MF; ++mf) af[pb][q][mf] = ldsread16((a_rd ^ ((k & 1) * 64)) + (s % NA) * ASTG + (k >> 1) * ASUB + mf * 2048);
#pragma unroll
      for (int nf = 0; nf < NF; ++nf) {
        const unsigned o = (BL<BN>::PADDED ? b_rd[0] + nf * 32 : b_rd[nf]) + (s % NB) * BSTG + (k >> 1) * BSUB + (k & 1) * 8 * BL<BN>::GS;
        bfr[pb][q][nf] = CAT8(trread(o), trread(o + BL<BN>::GS));
      }
    }
  };
  lds_bar();
  rdfrags(0);
#pragma unroll
  for (int gi = 0; gi < NG; ++gi) {
    if (gi + 1 < NG) {
      if ((gi + 1) % GPS == 0) lds_bar();
      SBAR();
      rdfrags(gi + 1);
    }
    const int pb = gi & 1;
#pragma unroll
    for (int q = 0; q < PD; ++q)
#pragma unroll
      for (int nf = 0; nf < NF; ++nf)
#pragma unroll
        for (int mf = 0; mf < MF; ++mf) acc[mf][nf] = MFMA16(bfr[pb][q][nf], af[pb][q][mf], acc[mf][nf]);
    if (gi + 1 < NG) {
      constexpr int NM = PD * MF * NF, ND = PD * (MF + 2 * NF);
      constexpr int PER = (ND + NM - 1) / NM;
#pragma unroll
      for (int i = 0; i < NM; ++i) {
        __builtin_amdgcn_sched_group_barrier(0x100, PER, 0);
        __builtin_amdgcn_sched_group_barrier(0x8, 1, 0);
      }
    }
#pragma unroll
    for (int mf = 0; mf < MF; ++mf)
#pragma unroll
      for (int nf = 0; nf < NF; ++nf) PIN(acc[mf][nf]);
  }
  lds_bar();
  if (LN) {
    f32x4 c0[NF];
    float rstd[MF], nmr[MF];
#pragma unroll
    for (int nf = 0; nf < NF; ++nf) c0[nf] = *(const f32x4*)(csb + (f0 + nf) * 16 + g * 4);
    if (!csin && m0 == 0 && wm == 0 && l15 == 0) {
#pragma unroll
      for (int nf = 0; nf < NF; ++nf) st16(csio + ncol + nf * 16 + g * 4, __builtin_bit_cast(u32x4, c0[nf]));
    }
#pragma unroll
    for (int mf = 0; mf < MF; ++mf) {
      const f32x2 a = *(const f32x2*)(st + (wm * (MF * 16) + mf * 16 + l15) * 2);
      const float mean = a[0] * (1.f / KTOT);
      const float var = fmaxf(a[1] * (1.f / KTOT) - mean * mean, 0.f);
      rstd[mf] = __builtin_amdgcn_rsqf(var + 1e-5f);
      nmr[mf] = -mean * rstd[mf];
    }
    const int tk = trow + (g & 1) * 16;
    if (EPI == EPI_QKV) {
      const unsigned lin = tk * 16 + (g >> 1) * 8;
      const unsigned kof = (tk >> 5) * 512 + (g >> 1) * 256 + (tk & 31) * 8;
#pragma unroll
      for (int nf = 0; nf < NF; ++nf) {
        const int hh = (ncol >> 4) + nf;
        const bool isq = hh < 32, isk = (hh >> 5) == 1;
        const float sc = isq ? 0.25f * 1.4426950408889634f : 1.f;
        u16* base = out + (size_t)hh * (SEQ * 16);
        u32x2 o[MF];
#pragma unroll
        for (int mf = 0; mf < MF; ++mf) {
          const f32x4 y = acc[mf][nf] * (rstd[mf] * sc) + c0[nf] * (nmr[mf] * sc);
          o[mf] = u32x2{cvtpk(y[0], y[1]), cvtpk(y[2], y[3])};
        }
        st16(base + (isk ? kof : lin), pair16(o[0], o[MF - 1]));
      }
    } else {
      u32x2 o[MF][NF];
#pragma unroll
      for (int nf = 0; nf < NF; ++nf) {
        const u32x2 bb = *(const u32x2*)(smem + EOFF + 16384 + ((f0 + nf) * 16 + g * 4) * 2);
        const f32x4 bias = {bflo(bb[0]), bfhi(bb[0]), bflo(bb[1]), bfhi(bb[1])};
#pragma unroll
        for (int mf = 0; mf < MF; ++mf) {
          const f32x4 h = acc[mf][nf] * rstd[mf] + (c0[nf] * nmr[mf] + bias);
          const f32x4 t = h * 1.4426950408889634f;
          f32x4 u, rc;
#pragma unroll
          for (int j = 0; j < 4; ++j) u[j] = __builtin_amdgcn_exp2f(t[j]);
          const f32x4 d = u * (u * -0.5f - 1.f) - 1.f;
#pragma unroll
          for (int j = 0; j < 4; ++j) rc[j] = __builtin_amdgcn_rcpf(d[j]);
          const f32x4 a = h * rc + h;
          o[mf][nf] = u32x2{cvtpk(a[0], a[1]), cvtpk(a[2], a[3])};
          if (nf & 1) st16pair(out + (size_t)(trow + mf * 16) * N + ncol + (nf - 1) * 16, g, o[mf][nf - 1], o[mf][nf]);
        }
      }
      if (NF & 1) st16(out + (size_t)tk * N + ncol + (NF - 1) * 16 + (g >> 1) * 8, pair16(o[0][NF - 1], o[MF - 1][NF - 1]));
    }
  } else {
    float s1 = 0.f, s2 = 0.f;
    u32x2 o[NF];
#pragma unroll
    for (int nf = 0; nf < NF; ++nf) {
      const u32x2 rr = *(const u32x2*)(smem + EOFF + (wm * 16 + l15) * 128 + ((((f0 + nf) * 2 + (g >> 1)) ^ (l15 >> 1)) * 16) + (g & 1) * 8);
      const f32x4 r = acc[0][nf];
      o[nf] = u32x2{cvtpk(r[0] + bflo(rr[0]), r[1] + bfhi(rr[0])), cvtpk(r[2] + bflo(rr[1]), r[3] + bfhi(rr[1]))};
      const float e0 = bflo(o[nf][0]), e1 = bfhi(o[nf][0]), e2 = bflo(o[nf][1]), e3 = bfhi(o[nf][1]);
      s1 += (e0 + e1) + (e2 + e3);
      s2 += (e0 * e0 + e1 * e1) + (e2 * e2 + e3 * e3);
    }
    st16pair(out + (size_t)trow * N + ncol, g, o[0], o[1]);
    s1 = sum32(sum16(s1)); s2 = sum32(sum16(s2));
    if (g == 0) st8(stats + (trow >> 7) * 4096 + (ncol >> 5) * 256 + (trow & 127) * 2, f32x2{s1, s2});
  }
  if (!FIRST) signal_done(smem, scnt, lane, EPI == EPI_QKV ? 48 : 16);
}

template <int BM, int BN, int KTOT, int N, int NA, int NB, int PB, int SUB, int PD, int EPI, bool FIRST>
DI void gemm_phase(char* smem, const u16* Ap, const u16* Bp, const u16* aux, u16* out, float* stats, int m0, int n0,
                   unsigned* wcnt, unsigned wtarget, unsigned* scnt, unsigned ph, unsigned narr, float* csio, bool csin) {
  asm volatile("" : "+s"(Ap), "+s"(Bp), "+s"(aux), "+s"(out), "+s"(stats), "+s"(m0), "+s"(n0));
  constexpr int NK = KTOT / (64 * SUB);
  constexpr int ASUB = BM * 128, BSUB = BL<BN>::STAGE, ASTG = ASUB * SUB, BSTG = BSUB * SUB, BOFF = NA * ASTG;
  static_assert(BOFF + NB * BSTG <= EOFF, "rings exceed LDS");
  constexpr int LA = BM / 16;
  constexpr int LB = BL<BN>::NI / 2;
  constexpr int LM = LA > LB ? LA : LB;
  constexpr bool LN = (EPI != EPI_RES);
  constexpr bool AHM = (EPI == EPI_RES && KTOT == 512);
  constexpr int NE = (EPI == EPI_RES) ? 2 : (FIRST ? 0 : (EPI == EPI_FFN1 ? 5 : 4));
  using SA = Sched<NK, NA, 2, LA * SUB, NE>;
  using SB = Sched<NK, NB, PB, LB * SUB, 0>;
  const unsigned lds0 = (unsigned)(size_t)(LDSP(char))smem;
  int tid = threadIdx.x;
  asm volatile("" : "+v"(tid));
  const int lane = tid & 63;
  const int wid = __builtin_amdgcn_readfirstlane(tid >> 6);

  if (wid < 8) {
    if (!FIRST && wid == 0) { wait_cnt(wcnt, wtarget, lane, 1); *SW(8) = ph; }
    constexpr int NF0 = (BN / 16 + 1) / 2, NF1 = BN / 16 - NF0;
    if (NF0 == NF1 || wid < 4) gemm_compute<BM, BN, KTOT, N, NA, NB, SUB, PD, EPI, FIRST, NF0>(smem, out, stats, m0, n0, wid & 3, (wid >> 2) * NF0, lane, scnt, csio, csin);
    else gemm_compute<BM, BN, KTOT, N, NA, NB, SUB, PD, EPI, FIRST, NF1>(smem, out, stats, m0, n0, wid & 3, NF0, lane, scnt, csio, csin);
    return;
  }

  float* csb = (float*)(smem + XOFF);
  float* st = csb + 256;
  const int l15 = lane & 15, g = lane >> 4;
  const int w4 = wid - 8, w2 = w4 & 1;
  const bool isA = w4 < 2;
  unsigned off[LM];
#pragma unroll
  for (int i = 0; i < LM; ++i) {
    if (isA) {
      const int row = (w2 * LA + i) * 8 + (lane >> 3), ch = (lane & 7) ^ ((row >> 1) & 7);
      off[i] = AHM ? (unsigned)((ch >> 1) * 65536 + row * 32 + (ch & 1) * 16) : (unsigned)(row * KTOT * 2 + ch * 16);
    } else {
      constexpr int CPR = BN / 8;
      const int row = (w2 * LB + i) * (BL<BN>::PADDED ? 4 : 64 / CPR) + lane / CPR;
      const int ch = BL<BN>::PADDED ? lane % CPR : (lane % CPR) ^ (BL<BN>::sw(row) << 1);
      off[i] = (unsigned)(row * N * 2 + ch * 16);
    }
  }
  const char* abase = (const char*)Ap + (AHM ? (size_t)m0 * 32 : (size_t)m0 * KTOT * 2);
  const char* bbase = (const char*)Bp + (size_t)n0 * 2;
  auto extras = [&]() {
    if (EPI == EPI_RES) {
#pragma unroll
      for (int i = 0; i < 2; ++i) {
        const int row = (w4 * 2 + i) * 8 + (lane >> 3), ch = (lane & 7) ^ ((row >> 1) & 7);
        glds16s((const char*)aux + ((size_t)m0 * N + n0) * 2, (unsigned)(row * N * 2 + ch * 16), lds0 + EOFF + (w4 * 2 + i) * 1024);
      }
    } else if (!FIRST) {
#pragma unroll
      for (int i = 0; i < 4; ++i) glds16s((const char*)stats + (size_t)m0 * 128 + w4 * 256 + i * 4096, g * 1024 + l15 * 16, lds0 + EOFF + (w4 * 4 + i) * 1024);
      if (EPI == EPI_FFN1) glds16m<32>((const char*)aux + n0 * 2, (l15 < BN / 8 ? l15 : 0) * 16, lds0 + EOFF + 16384);
    }
  };
  auto issueA = [&](int s) {
#pragma unroll
    for (int u = 0; u < SUB; ++u)
#pragma unroll
      for (int i = 0; i < LA; ++i)
        glds16s(abase + (size_t)(s * SUB + u) * (AHM ? 4 * 65536 : 128), off[i], lds0 + (s % NA) * ASTG + u * ASUB + (w2 * LA + i) * 1024);
    if (s == NK - 1) extras();
  };
  auto issueB = [&](int s) {
#pragma unroll
    for (int u = 0; u < SUB; ++u) {
      const unsigned slot = lds0 + BOFF + (s % NB) * BSTG + u * BSUB;
      const char* src = bbase + (size_t)(s * SUB + u) * 64 * N * 2;
#pragma unroll
      for (int i = 0; i < LB; ++i) {
        if (BL<BN>::PADDED) glds16m<BL<BN>::PADDED ? 4 * (BN / 8) : 64>(src, off[i], slot + (w2 * LB + i) * BL<BN>::GS);
        else glds16(src, off[i], slot + (w2 * LB + i) * 1024);
      }
    }
  };
  f32x2 csv = {0.f, 0.f};
  if (LN && csin && w4 == 3 && lane < BN / 2) csv = ld8s(csio + n0 + lane * 2);
  if (isA) {
    if (!FIRST) lds_wait(smem, 8, ph);
#pragma unroll
    for (int s = 0; s < SA::up(0); ++s) issueA(s);
  } else {
    if (!FIRST) lds_wait(smem, 0, narr);
#pragma unroll
    for (int s = 0; s < SB::up(0); ++s) issueB(s);
  }
  const bf16x8 ONES = __builtin_bit_cast(bf16x8, u32x4{0x3f803f80u, 0x3f803f80u, 0x3f803f80u, 0x3f803f80u});
  const unsigned c_rd0 = lds0 + BOFF + BL<BN>::frag(w4, lane), c_rd1 = lds0 + BOFF + BL<BN>::frag(w4 + 4, lane);
  const unsigned a_rd = lds0 + (w4 * 32 + l15) * 128 + ((g ^ (l15 >> 1)) * 16);
  f32x4 cs0 = {0.f, 0.f, 0.f, 0.f}, cs1 = cs0, rs[2] = {cs0, cs0}, rq[2] = {cs0, cs0};
#pragma unroll
  for (int j = 0; j < NK; ++j) {
    if (isA) {
      wait_bar_n(SA::behind(j));
#pragma unroll
      for (int s = SA::up(j); s < SA::up(j + 1); ++s) issueA(s);
    } else {
      wait_bar_n(SB::behind(j) + (j == NK - 1 ? NE : 0));
#pragma unroll
      for (int s = SB::up(j); s < SB::up(j + 1); ++s) issueB(s);
      if (j == NK - 2) extras();
    }
    if (LN && !csin) {
#pragma unroll
      for (int ks = 0; ks < 2; ++ks) {
        const unsigned o = (j % NB) * BSTG + ks * 8 * BL<BN>::GS;
        cs0 = MFMA16(CAT8(trread(c_rd0 + o), trread(c_rd0 + o + BL<BN>::GS)), ONES, cs0);
        cs1 = MFMA16(CAT8(trread(c_rd1 + o), trread(c_rd1 + o + BL<BN>::GS)), ONES, cs1);
        if (FIRST) {
#pragma unroll
          for (int mf = 0; mf < 2; ++mf) {
            const bf16x8 a = ldsread16((a_rd ^ (ks * 64)) + (j % NA) * ASTG + mf * 2048);
            rs[mf] = MFMA16(ONES, a, rs[mf]);
            rq[mf] = MFMA16(a, a, rq[mf]);
          }
        }
      }
      PIN(cs0); PIN(cs1);
      if (FIRST) { PIN(rs[0]); PIN(rs[1]); PIN(rq[0]); PIN(rq[1]); }
    }
  }
  if (LN) {
    if (csin) {
      if (w4 == 3 && lane < BN / 2) *(f32x2*)(csb + lane * 2) = csv;
    } else if (l15 == 0) {
      *(f32x4*)(csb + w4 * 16 + g * 4) = cs0;
      *(f32x4*)(csb + 64 + w4 * 16 + g * 4) = cs1;
    }
    if (FIRST) {
#pragma unroll
      for (int mf = 0; mf < 2; ++mf) {
        const int jj = l15 & 3;
        const float d = (jj == 0) ? rq[mf][0] : (jj == 1) ? rq[mf][1] : (jj == 2) ? rq[mf][2] : rq[mf][3];
        if (g == (l15 >> 2)) *(f32x2*)(st + (w4 * 32 + mf * 16 + l15) * 2) = f32x2{rs[mf][0], d};
      }
    } else {
      wait_vm<0>();
      f32x4 v = *(const f32x4*)(smem + EOFF + (w4 * 4) * 1024 + lane * 16);
#pragma unroll
      for (int i = 1; i < 4; ++i) v += *(const f32x4*)(smem + EOFF + (w4 * 4 + i) * 1024 + lane * 16);
#pragma unroll
      for (int i = 0; i < 4; ++i) v[i] = sum32(sum16(v[i]));
      if (g == 0) *(f32x4*)(st + (w4 * 32 + 2 * l15) * 2) = v;
    }
  }
  wait_bar<0>();
}

constexpr int AT_K = 0, AT_V = 65536, AT_ONE = 131072, AT_Q = AT_ONE + 6144, AT_PF = AT_Q + 8 * 9 * 256;
static_assert(AT_PF + 1024 <= XOFF, "attention LDS");

#define ATT_HALF(SC, OO, VC, MID)                                                                            \
  {                                                                                                          \
    _Pragma("unroll") for (int i = 0; i < 4; ++i) SC[i] = __builtin_amdgcn_exp2f(SC[i]);                     \
    SBAR(); MID; SBAR();                                                                                     \
    _Pragma("unroll") for (int i = 4; i < 9; ++i) SC[i] = __builtin_amdgcn_exp2f(SC[i]);                     \
    u32x4 p0 = {cvtpk(SC[0], SC[1]), cvtpk(SC[2], SC[3]), cvtpk(SC[4], SC[5]), cvtpk(SC[6], SC[7])};         \
    PIN(p0); SBAR();                                                                                         \
    SC[9] = __builtin_amdgcn_exp2f(SC[9]); SBAR();                                                           \
    OO = MFMA32(CAT8(VC[0], VC[1]), __builtin_bit_cast(bf16x8, p0), OO); SBAR();                             \
    _Pragma("unroll") for (int i = 10; i < 16; ++i) SC[i] = __builtin_amdgcn_exp2f(SC[i]);                   \
    u32x4 p1 = {cvtpk(SC[8], SC[9]), cvtpk(SC[10], SC[11]), cvtpk(SC[12], SC[13]), cvtpk(SC[14], SC[15])};   \
    OO = MFMA32(CAT8(VC[2], VC[3]), __builtin_bit_cast(bf16x8, p1), OO); SBAR();                             \
  }
#define ATT_TILE(VC, VN, T)                                                                                  \
  ATT_HALF(s0, o0, VC, { s1 = MFMA32(kf, qf1, cm1); PIN(s1); kf = ldsread16(kaddr + ((T) + 1) * 1024);       \
       VN[0] = trread(vaddr + ((T) + 1) * 1024); VN[1] = trread(vaddr + ((T) + 1) * 1024 + 256); })          \
  ATT_HALF(s1, o1, VC, { s0 = MFMA32(kf, qf0, cm0); PIN(s0);                                                 \
       VN[2] = trread(vaddr + ((T) + 1) * 1024 + 512); VN[3] = trread(vaddr + ((T) + 1) * 1024 + 768); })

template <bool FAST>
DI void attn_pass(unsigned lds0, int lane, int ks, const bf16x8 qf0, const bf16x8 qf1, float nm0, float nm1, f32x16& o0, f32x16& o1) {
  const int h = lane >> 5;
  unsigned kaddr = lds0 + AT_K + ks * 32768 + h * 512 + (lane & 31) * 16;
  const bool cl = (lane >> 4) & 1;
  const int qq = (lane & 15) >> 2, pp = lane & 3;
  unsigned vaddr = cl ? lds0 + AT_ONE + (4 * (h ^ 1) + qq) * 32 + pp * 8 : lds0 + AT_V + ks * 32768 + (4 * h + qq) * 32 + pp * 8;
  const unsigned vstep = cl ? 0u : 4096u;
  f32x16 cm0, cm1, s0, s1;
  s16x4 vA[4], vB[4];
#pragma unroll
  for (int i = 0; i < 16; ++i) { cm0[i] = FAST ? 0.f : nm0; cm1[i] = FAST ? 0.f : nm1; o0[i] = 0.f; o1[i] = 0.f; }
  bf16x8 kf = ldsread16(kaddr);
  s0 = MFMA32(kf, qf0, cm0);
  vA[0] = trread(vaddr); vA[1] = trread(vaddr + 256); vA[2] = trread(vaddr + 512); vA[3] = trread(vaddr + 768);
  for (int it = 0; it < 8; ++it) {
    PIN(kaddr); PIN(vaddr);
    ATT_TILE(vA, vB, 0) ATT_TILE(vB, vA, 1) ATT_TILE(vA, vB, 2) ATT_TILE(vB, vA, 3)
    kaddr += 4096; vaddr += vstep;
    if (it == 0) lds_bar();
  }
}

DI void attn_phase(char* smem, const u16* QKV, u16* O, const u16* wo, const u16* w1, const u16* w2, unsigned* cq, bool wait, unsigned* scnt) {
  asm volatile("" : "+s"(QKV), "+s"(O));
  const unsigned lds0 = (unsigned)(size_t)(LDSP(char))smem;
  int tid = threadIdx.x;
  asm volatile("" : "+v"(tid));
  const int lane = tid & 63;
  const int wid = __builtin_amdgcn_readfirstlane(tid >> 6);
  const int bid = blockIdx.x;
  const int head = (bid & 7) * 4 + ((bid >> 3) & 3), qb = bid >> 5;
  if (wid >= 8) {
    const int w4 = wid - 8;
    const unsigned l16 = lane * 16;
#pragma unroll
    for (int i = 0; i < 2; ++i) { const int t = (w4 * 2 + i) * 64 + lane; if (t < 384) *(u32x4*)(smem + AT_ONE + t * 16) = u32x4{(t & 1) ? 0u : 0x3f80u, 0u, 0u, 0u}; }
    if (tid == 767) *SW(4) = 0u;
    if (wait) lds_wait(smem, 8, 5u);
    const char* qsrc = (const char*)QKV + ((size_t)head * SEQ + qb * 256) * 32 + w4 * 2048;
    const char* src = (const char*)QKV + (size_t)(32 + 32 * (w4 >> 1) + head) * 65536 + (w4 & 1) * 32768;
    const unsigned dst = lds0 + (w4 >> 1) * 65536 + (w4 & 1) * 32768;
    glds16s(qsrc, l16, lds0 + AT_Q + w4 * 2048); glds16s(qsrc + 1024, l16, lds0 + AT_Q + w4 * 2048 + 1024);
#pragma unroll
    for (int i = 0; i < 8; ++i) glds16s(src + i * 1024, l16, dst + i * 1024);
    wait_bar<0>();
#pragma unroll
    for (int i = 8; i < 32; ++i) glds16s(src + i * 1024, l16, dst + i * 1024);
    wait_bar<0>();
    {
      const int u = (bid >> 3) * 4 + w4;
      const char* p1 = (u < 28) ? (const char*)wo + u * 18432 : (const char*)w1 + (u - 28) * 18432;
      const int n1 = (u < 28) ? (u == 27 ? 26624 : 18432) : (u == 127 ? 10240 : 18432);
      const char* p2 = (const char*)w2 + (size_t)(bid * 4 + w4) * 1792;
      glds16(p2, l16, lds0 + AT_PF); glds16m<48>(p2 + 1024, l16, lds0 + AT_PF);
      for (int off = 0; off < n1; off += 1024) {
        glds16(p1 + off, l16, lds0 + AT_PF);
        if ((off & 3072) == 3072) __builtin_amdgcn_s_sleep(40);
      }
      wait_vm<0>();
    }
    lds_bar();
    if (__builtin_amdgcn_readfirstlane(*SW(4)) != 0u) { lds_bar(); lds_bar(); }
    return;
  }
  const int wq = wid & 3, ks = wid >> 2;
  if (wait && wid == 0) {
    const int hh = (lane < 3 ? lane : 0) * 32 + head;
    wait_cnt(cq + ((hh / 6) * 64 + (hh % 6) * 8 + qb) * 32, 16, lane, 3);
    *SW(8) = 5u;
  }
  lds_bar();
  const bf16x8 qf0 = ldsread16(lds0 + AT_Q + wq * 2048 + (lane & 31) * 32 + (lane >> 5) * 16);
  const bf16x8 qf1 = ldsread16(lds0 + AT_Q + wq * 2048 + 1024 + (lane & 31) * 32 + (lane >> 5) * 16);
  f32x16 o0, o1;
  attn_pass<true>(lds0, lane, ks, qf0, qf1, 0.f, 0.f, o0, o1);
  float* xb = (float*)(smem + AT_Q);
  if (lane < 32 && !(o0[8] >= 8.67e-19f && o0[8] <= 1.26e30f && o1[8] >= 8.67e-19f && o1[8] <= 1.26e30f)) *SW(4) = 1u;
#pragma unroll
  for (int i = 0; i < 9; ++i) xb[(wid * 9 + i) * 64 + lane] = ks ? o0[i] : o1[i];
  lds_bar();
  if (__builtin_amdgcn_readfirstlane(*SW(4)) != 0u) {
    float m0 = -3.0e38f, m1 = -3.0e38f;
    const unsigned kaddr = lds0 + AT_K + (lane >> 5) * 512 + (lane & 31) * 16;
    f32x16 z;
#pragma unroll
    for (int i = 0; i < 16; ++i) z[i] = 0.f;
    for (int t = 0; t < 64; ++t) {
      const bf16x8 kf = ldsread16(kaddr + t * 1024);
      const f32x16 sa = MFMA32(kf, qf0, z), sb = MFMA32(kf, qf1, z);
#pragma unroll
      for (int i = 0; i < 16; ++i) { m0 = fmaxf(m0, sa[i]); m1 = fmaxf(m1, sb[i]); }
    }
    m0 = max32(m0); m1 = max32(m1);
    attn_pass<false>(lds0, lane, ks, qf0, qf1, -m0, -m1, o0, o1);
#pragma unroll
    for (int i = 0; i < 9; ++i) xb[(wid * 9 + i) * 64 + lane] = ks ? o0[i] : o1[i];
    lds_bar();
  }
  f32x16 o = ks ? o1 : o0;
#pragma unroll
  for (int i = 0; i < 9; ++i) o[i] += xb[((wid ^ 4) * 9 + i) * 64 + lane];
  auto lr = __builtin_amdgcn_permlane32_swap(__float_as_uint(o[8]), __float_as_uint(o[8]), false, false);
  const float rl = __builtin_amdgcn_rcpf(__uint_as_float(lr[0]));
  unsigned x0 = cvtpk(o[0] * rl, o[1] * rl), x1 = cvtpk(o[2] * rl, o[3] * rl);
  unsigned y0 = cvtpk(o[4] * rl, o[5] * rl), y1 = cvtpk(o[6] * rl, o[7] * rl);
  auto r0 = __builtin_amdgcn_permlane32_swap(x0, y0, false, false);
  auto r1 = __builtin_amdgcn_permlane32_swap(x1, y1, false, false);
  const int q0 = qb * 256 + wq * 64 + ks * 32;
  st16(O + ((size_t)head * SEQ + q0 + (lane & 31)) * 16 + (lane >> 5) * 8, u32x4{r0[0], r1[0], r0[1], r1[1]});
  signal_done(smem, scnt, lane, 32);
}

constexpr size_t MB = 1 << 20;
constexpr size_t WS_QKV = 0, WS_O = 12 * MB, WS_X1 = 16 * MB, WS_X2 = 20 * MB, WS_H = 22 * MB, WS_ST = 38 * MB, WS_CNT = 39 * MB, WS_CS = 40 * MB;
constexpr int C_A = 0, C_1 = 256, C_2 = 512, C_3 = 768, C_LAYER = 1024, C_Q = 2048, C_TOTAL = 3072;
constexpr int NF1T = FF / F1BN;

struct Params {
  const u16 *x, *wqkv, *wo, *w1, *b1, *w2;
  u16* out;
  char* ws;
};

extern "C" __global__ __launch_bounds__(768, 3) void k_mega(Params p) {
  __shared__ __attribute__((aligned(256))) char smem[LDS_BYTES];
  if (threadIdx.x == 0) { *SW(0) = 0u; *SW(8) = 0u; }
  if (__builtin_amdgcn_readfirstlane(threadIdx.x) >= 512) __builtin_amdgcn_s_setprio(3);
  const int bid = blockIdx.x, idx = bid >> 3;
  const int team = (bid & 7) * 2 + (idx >> 4), mem = idx & 15;
  unsigned narr = 0;
  for (int layer = 0; layer < 2; ++layer) {
    asm volatile("" : "+s"(p.ws));
    unsigned* cnt = (unsigned*)(p.ws + WS_CNT);
    unsigned* cl = cnt + layer * C_LAYER * 32;
    u16* O = (u16*)(p.ws + WS_O + layer * 2 * MB);
    u16* X1 = (u16*)(p.ws + WS_X1 + layer * 2 * MB);
    u16* X2 = (u16*)(p.ws + WS_X2);
    u16* H = (u16*)(p.ws + WS_H + layer * 8 * MB);
    float* st1 = (float*)(p.ws + WS_ST + layer * 2 * 262144);
    float* st2 = st1 + 65536;
    float* cs = (float*)(p.ws + WS_CS);
    const int me = team * 16 + mem;
    attn_phase(smem, (const u16*)(p.ws + WS_QKV + layer * 6 * MB), O, p.wo, p.w1, p.w2, cnt + C_Q * 32, layer != 0, cl + (C_A + (bid >> 5) * 32) * 32);
    narr += 8;
    gemm_phase<64, 64, 512, 512, 4, 4, 3, 2, 2, EPI_RES, false>(smem, O, p.wo, layer ? X2 : p.x, X1, st1, team * 128 + (mem >> 3) * 64, (mem & 7) * 64,
                                                                 cl + (C_A + me) * 32, 32, cl + (C_1 + team * 16) * 32, 1 + layer * 8, narr, nullptr, false);
    narr += 8;
    if (mem < NF1T) {
      gemm_phase<128, F1BN, 512, 1792, 3, 5, 3, 1, 2, EPI_FFN1, false>(smem, X1, p.w1, p.b1, H, st1, team * 128, mem * F1BN,
                                                                       cl + (C_1 + me) * 32, 16, cl + (C_2 + team * 16) * 32, 2 + layer * 8, narr, cs + 2048, layer != 0);
      narr += 8;
    }
    gemm_phase<64, 64, 1792, 512, 3, 5, 3, 2, 2, EPI_RES, false>(smem, H, p.w2, X1, layer ? p.out : X2, st2, team * 128 + (mem >> 3) * 64, (mem & 7) * 64,
                                                                  cl + (C_2 + me) * 32, NF1T, cl + (C_3 + team * 16) * 32, 3 + layer * 8, narr, nullptr, false);
    narr += 8;
    if (layer == 0) {
      gemm_phase<128, 96, 512, 1536, 3, 7, 4, 1, 2, EPI_QKV, false>(smem, X2, p.wqkv, nullptr, (u16*)(p.ws + WS_QKV + 6 * MB), st2, team * 128, mem * 96,
                                                                    cl + (C_3 + me) * 32, 16, cnt + (C_Q + mem * 64) * 32, 4, narr, cs, true);
      narr += 8;
    }
  }
}
extern "C" __global__ void k_zend() {}

extern "C" __global__ __launch_bounds__(768, 3) void k_qkv1(Params p) {
  __shared__ __attribute__((aligned(256))) char smem[LDS_BYTES];
  const int bid = blockIdx.x, xcd = bid & 7, idx = bid >> 3;
  if (__builtin_amdgcn_readfirstlane(threadIdx.x) >= 512) __builtin_amdgcn_s_setprio(3);
  if (threadIdx.x < C_TOTAL / 256) *(unsigned*)(p.ws + WS_CNT + (bid * (C_TOTAL / 256) + threadIdx.x) * 128) = 0u;
  unsigned codev = 0;
  {
    unsigned long long lo, hi;
    asm volatile("s_getpc_b64 s[20:21]\n\ts_add_u32 s20, s20, k_mega@rel32@lo+4\n\ts_addc_u32 s21, s21, k_mega@rel32@hi+12\n\ts_mov_b64 %0, s[20:21]\n\t"
                 "s_getpc_b64 s[20:21]\n\ts_add_u32 s20, s20, k_zend@rel32@lo+4\n\ts_addc_u32 s21, s21, k_zend@rel32@hi+12\n\ts_mov_b64 %1, s[20:21]"
                 : "=s"(lo), "=s"(hi)::"s20", "s21", "scc");
    const unsigned long long a = lo + (unsigned long long)(idx * 2048 + threadIdx.x * 4);
    if (threadIdx.x < 512 && a + 4 <= hi) codev = *(const __attribute__((address_space(1))) unsigned*)a;
  }
  gemm_phase<128, 96, 512, 1536, 3, 3, 2, 1, 2, EPI_QKV, true>(smem, p.x, p.wqkv, nullptr, (u16*)(p.ws + WS_QKV), nullptr,
                                                               ((xcd >> 1) * 4 + idx / 8) * 128, ((xcd & 1) * 8 + idx % 8) * 96,
                                                               nullptr, 0, nullptr, 0, 0, (float*)(p.ws + WS_CS), false);
  asm volatile("" ::"v"(codev));
}

extern "C" void kernel_launch(void* const* d_in, const int* in_sizes, int n_in, void* d_out, int out_size, void* d_ws,
                              size_t ws_size, hipStream_t stream) {
  Params p;
  p.x = (const u16*)d_in[0]; p.wqkv = (const u16*)d_in[1]; p.wo = (const u16*)d_in[2];
  p.w1 = (const u16*)d_in[4]; p.b1 = (const u16*)d_in[5]; p.w2 = (const u16*)d_in[6];
  p.out = (u16*)d_out; p.ws = (char*)d_ws + 100 * MB;
  k_qkv1<<<256, 768, 0, stream>>>(p);
  k_mega<<<256, 768, 0, stream>>>(p);
}
```

```cpp
#include <hip/hip_runtime.h>
#include <hip/hip_bf16.h>
#include <stdint.h>

typedef short bf16x8 __attribute__((ext_vector_type(8)));
typedef short s16x4 __attribute__((ext_vector_type(4)));
typedef __bf16 bf16x2 __attribute__((ext_vector_type(2)));
typedef float f32x2 __attribute__((ext_vector_type(2)));
typedef float f32x4 __attribute__((ext_vector_type(4)));
typedef float f32x16 __attribute__((ext_vector_type(16)));
typedef unsigned u32x2 __attribute__((ext_vector_type(2)));
typedef unsigned u32x4 __attribute__((ext_vector_type(4)));
typedef unsigned short u16;
typedef __attribute__((address_space(1))) unsigned gu32;
#define LDSP(T) __attribute__((address_space(3))) T*
#define DI __device__ __forceinline__
#define SBAR() __builtin_amdgcn_sched_barrier(0)
#define PIN(x) asm volatile("" : "+v"(x))
#define MFMA16(a, b, c) __builtin_amdgcn_mfma_f32_16x16x32_bf16(a, b, c, 0, 0, 0)
#define MFMA32(a, b, c) __builtin_amdgcn_mfma_f32_32x32x16_bf16(a, b, c, 0, 0, 0)
#define CAT8(a, b) __builtin_shufflevector(a, b, 0, 1, 2, 3, 4, 5, 6, 7)

#define EARLYK 1
#define F1BN 128

constexpr int SEQ = 2048, DM = 512, FF = 1792;

DI unsigned cvtpk(float lo, float hi) { return __builtin_bit_cast(unsigned, __builtin_convertvector(f32x2{lo, hi}, bf16x2)); }
DI float bflo(unsigned u) { return __uint_as_float(u << 16); }
DI float bfhi(unsigned u) { return __uint_as_float(u & 0xffff0000u); }

DI void glds16(const void* sbase, unsigned voff, unsigned lds_addr) {
  unsigned sv;
  asm volatile("s_mov_b32 %0, m0\n\ts_mov_b32 m0, %3\n\ts_nop 0\n\tglobal_load_lds_dwordx4 %1, %2\n\ts_mov_b32 m0, %0"
               : "=&s"(sv) : "v"(voff), "s"(sbase), "s"(lds_addr) : "memory");
}
DI void glds16s(const void* sbase, unsigned voff, unsigned lds_addr) {
  unsigned sv;
  asm volatile("s_mov_b32 %0, m0\n\ts_mov_b32 m0, %3\n\ts_nop 0\n\tglobal_load_lds_dwordx4 %1, %2 sc1\n\ts_mov_b32 m0, %0"
               : "=&s"(sv) : "v"(voff), "s"(sbase), "s"(lds_addr) : "memory");
}
template <int NL> DI void glds16m(const void* sbase, unsigned voff, unsigned lds_addr) {
  unsigned sv;
  asm volatile("s_mov_b32 %0, m0\n\ts_mov_b32 m0, %3\n\ts_mov_b32 exec_hi, %4\n\tglobal_load_lds_dwordx4 %1, %2\n\ts_mov_b32 exec_hi, -1\n\ts_mov_b32 m0, %0"
               : "=&s"(sv) : "v"(voff), "s"(sbase), "s"(lds_addr), "i"((unsigned)((1ull << (NL - 32)) - 1)) : "memory");
}
template <int N> DI void wait_bar() { asm volatile("s_waitcnt vmcnt(%0) lgkmcnt(0)\n\ts_barrier" ::"i"(N) : "memory"); }
DI void wait_bar_n(int n) { asm volatile("s_waitcnt vmcnt(%0) lgkmcnt(0)\n\ts_barrier" ::"i"(n) : "memory"); }
template <int N> DI void wait_vm() { asm volatile("s_waitcnt vmcnt(%0)" ::"i"(N) : "memory"); }
DI void lds_bar() { asm volatile("s_waitcnt lgkmcnt(0)\n\ts_barrier" ::: "memory"); }
DI void st16(void* ptr, u32x4 v) { asm volatile("global_store_dwordx4 %0, %1, off sc1\n\ts_nop 1" ::"v"(ptr), "v"(v) : "memory"); }
DI void st8(void* ptr, f32x2 v) { asm volatile("global_store_dwordx2 %0, %1, off sc1" ::"v"(ptr), "v"(v) : "memory"); }
DI f32x2 ld8s(const void* p) {
  return __builtin_bit_cast(f32x2, __hip_atomic_load((const __attribute__((address_space(1))) unsigned long long*)p, __ATOMIC_RELAXED, __HIP_MEMORY_SCOPE_AGENT));
}
DI u32x4 pair16(u32x2 a, u32x2 b) {
  auto r0 = __builtin_amdgcn_permlane16_swap(a[0], b[0], false, false);
  auto r1 = __builtin_amdgcn_permlane16_swap(a[1], b[1], false, false);
  return u32x4{r0[0], r1[0], r0[1], r1[1]};
}
DI void st16pair(u16* rowp, int g, u32x2 a, u32x2 b) { st16(rowp + (g & 1) * 16 + (g >> 1) * 8, pair16(a, b)); }
DI s16x4 trread(unsigned addr) { return __builtin_bit_cast(s16x4, __builtin_amdgcn_ds_read_tr16_b64_v4i16((LDSP(s16x4))(size_t)addr)); }
DI bf16x8 ldsread16(unsigned addr) { return *(const LDSP(bf16x8))(size_t)addr; }
DI float sum16(float v) { auto r = __builtin_amdgcn_permlane16_swap(__float_as_uint(v), __float_as_uint(v), false, false); return __uint_as_float(r[0]) + __uint_as_float(r[1]); }
DI float sum32(float v) { auto r = __builtin_amdgcn_permlane32_swap(__float_as_uint(v), __float_as_uint(v), false, false); return __uint_as_float(r[0]) + __uint_as_float(r[1]); }
DI float max32(float v) { auto r = __builtin_amdgcn_permlane32_swap(__float_as_uint(v), __float_as_uint(v), false, false); return fmaxf(__uint_as_float(r[0]), __uint_as_float(r[1])); }

constexpr int EOFF = 143360;
constexpr int XOFF = EOFF + 17408;
constexpr int SYNC = XOFF + 2048;
constexpr int LDS_BYTES = SYNC + 256;
#define SW(off) ((volatile LDSP(unsigned))((LDSP(char))smem + SYNC + (off)))

DI void signal_done(char* smem, unsigned* cnt, int lane, int nrep) {
  wait_vm<0>();
  unsigned old = 0;
  if (lane == 0) old = __hip_atomic_fetch_add((LDSP(unsigned))((LDSP(char))smem + SYNC), 1u, __ATOMIC_RELAXED, __HIP_MEMORY_SCOPE_WORKGROUP);
  if ((__builtin_amdgcn_readfirstlane(old) & 7u) == 7u && lane < nrep)
    __hip_atomic_fetch_add((gu32*)(cnt + lane * 32), 1u, __ATOMIC_RELAXED, __HIP_MEMORY_SCOPE_AGENT);
}
DI void wait_cnt(unsigned* c, unsigned target, int lane, int nc) {
  for (unsigned spins = 0; spins < 200000u; ++spins) {
    unsigned v = target;
    if (lane < nc) v = __hip_atomic_load((gu32*)c, __ATOMIC_RELAXED, __HIP_MEMORY_SCOPE_AGENT);
    if (__all(v >= target)) break;
  }
  asm volatile("" ::: "memory");
}
DI void lds_wait(char* smem, int off, unsigned v) {
  for (unsigned spins = 0; spins < 8000000u; ++spins) {
    if ((unsigned)__builtin_amdgcn_readfirstlane(*SW(off)) >= v) break;
    __builtin_amdgcn_s_sleep(1);
  }
  asm volatile("" ::: "memory");
}

enum { EPI_QKV = 0, EPI_FFN1 = 1, EPI_RES = 2 };

template <int BN> struct BL {
  static constexpr bool PADDED = (BN == 96 || BN == 112);
  static constexpr int RB = BN * 2;
  static constexpr int GS = BN == 96 ? 784 : BN == 112 ? 960 : 4 * RB;
  static constexpr int STAGE = 16 * GS;
  static constexpr int NI = PADDED ? 16 : (64 * RB / 1024);
  static DI int sw(int row) { return BN == 64 ? (((row >> 1) & 1) | (((row >> 3) & 1) << 1)) : ((row & 3) | (((row >> 3) & 1) << 2)); }
  static DI unsigned frag(int f, int lane) {
    const int g = lane >> 4, bq = (lane & 15) >> 2, bp = lane & 3, row = 8 * g + bq;
    return PADDED ? (2 * g) * GS + bq * RB + f * 32 + bp * 8 : row * RB + ((f ^ sw(row)) * 32) + bp * 8;
  }
};

template <int NK, int NS, int P0, int L, int NE> struct Sched {
  static constexpr int mn(int a, int b) { return a < b ? a : b; }
  static constexpr int up(int j) { return mn(mn(P0 + 2 * j, (j < 1 ? 1 : j) + NS - 1), NK); }
  static constexpr int behind(int j) { return (up(j) - 1 - j) * L + (up(j) == NK ? NE : 0); }
};

template <int BM, int BN, int KTOT, int N, int NA, int NB, int SUB, int PD, int EPI, bool FIRST, int NF>
DI void gemm_compute(char* smem, u16* out, float* stats, int m0, int n0, int wm, int f0, int lane, unsigned* scnt, float* csio, bool csin) {
  constexpr int NK = KTOT / (64 * SUB);
  constexpr int ASUB = BM * 128, BSUB = BL<BN>::STAGE, ASTG = ASUB * SUB, BSTG = BSUB * SUB, BOFF = NA * ASTG;
  constexpr int MF = BM / 64;
  constexpr bool LN = (EPI != EPI_RES);
  const unsigned lds0 = (unsigned)(size_t)(LDSP(char))smem;
  const float* csb = (const float*)(smem + XOFF);
  const float* st = csb + 256;
  const int l15 = lane & 15, g = lane >> 4;
  const int trow = m0 + wm * (MF * 16) + l15;
  const int ncol = n0 + f0 * 16;
  const unsigned a_rd = lds0 + (wm * (MF * 16) + l15) * 128 + ((g ^ (l15 >> 1)) * 16);
  unsigned b_rd[BL<BN>::PADDED ? 1 : NF];
#pragma unroll
  for (int nf = 0; nf < (BL<BN>::PADDED ? 1 : NF); ++nf) b_rd[nf] = lds0 + BOFF + BL<BN>::frag(f0 + nf, lane);
  f32x4 acc[MF][NF];
#pragma unroll
  for (int a = 0; a < MF; ++a)
#pragma unroll
    for (int b = 0; b < NF; ++b) acc[a][b] = f32x4{0.f, 0.f, 0.f, 0.f};
  constexpr int KS = 2 * SUB, GPS = KS / PD, NG = NK * GPS;
  bf16x8 af[2][PD][MF], bfr[2][PD][NF];
  auto rdfrags = [&](int gi) {
    const int pb = gi & 1, s = gi / GPS;
#pragma unroll
    for (int q = 0; q < PD; ++q) {
      const int k = (gi % GPS) * PD + q;
#pragma unroll
      for (int mf = 0; mf < MF; ++mf) af[pb][q][mf] = ldsread16((a_rd ^ ((k & 1) * 64)) + (s % NA) * ASTG + (k >> 1) * ASUB + mf * 2048);
#pragma unroll
      for (int nf = 0; nf < NF; ++nf) {
        const unsigned o = (BL<BN>::PADDED ? b_rd[0] + nf * 32 : b_rd[nf]) + (s % NB) * BSTG + (k >> 1) * BSUB + (k & 1) * 8 * BL<BN>::GS;
        bfr[pb][q][nf] = CAT8(trread(o), trread(o + BL<BN>::GS));
      }
    }
  };
  lds_bar();
  rdfrags(0);
#pragma unroll
  for (int gi = 0; gi < NG; ++gi) {
    if (gi + 1 < NG) {
      if ((gi + 1) % GPS == 0) lds_bar();
      SBAR();
      rdfrags(gi + 1);
    }
    const int pb = gi & 1;
#pragma unroll
    for (int q = 0; q < PD; ++q)
#pragma unroll
      for (int nf = 0; nf < NF; ++nf)
#pragma unroll
        for (int mf = 0; mf < MF; ++mf) acc[mf][nf] = MFMA16(bfr[pb][q][nf], af[pb][q][mf], acc[mf][nf]);
    if (gi + 1 < NG) {
      constexpr int NM = PD * MF * NF, ND = PD * (MF + 2 * NF);
      constexpr int PER = (ND + NM - 1) / NM;
#pragma unroll
      for (int i = 0; i < NM; ++i) {
        __builtin_amdgcn_sched_group_barrier(0x100, PER, 0);
        __builtin_amdgcn_sched_group_barrier(0x8, 1, 0);
      }
    }
#pragma unroll
    for (int mf = 0; mf < MF; ++mf)
#pragma unroll
      for (int nf = 0; nf < NF; ++nf) PIN(acc[mf][nf]);
  }
  lds_bar();
  if (LN) {
    f32x4 c0[NF];
    float rstd[MF], nmr[MF];
#pragma unroll
    for (int nf = 0; nf < NF; ++nf) c0[nf] = *(const f32x4*)(csb + (f0 + nf) * 16 + g * 4);
    if (!csin && m0 == 0 && wm == 0 && l15 == 0) {
#pragma unroll
      for (int nf = 0; nf < NF; ++nf) st16(csio + ncol + nf * 16 + g * 4, __builtin_bit_cast(u32x4, c0[nf]));
    }
#pragma unroll
    for (int mf = 0; mf < MF; ++mf) {
      const f32x2 a = *(const f32x2*)(st + (wm * (MF * 16) + mf * 16 + l15) * 2);
      const float mean = a[0] * (1.f / KTOT);
      const float var = fmaxf(a[1] * (1.f / KTOT) - mean * mean, 0.f);
      rstd[mf] = __builtin_amdgcn_rsqf(var + 1e-5f);
      nmr[mf] = -mean * rstd[mf];
    }
    const int tk = trow + (g & 1) * 16;
    if (EPI == EPI_QKV) {
      const unsigned lin = tk * 16 + (g >> 1) * 8;
      const unsigned kof = (tk >> 5) * 512 + (g >> 1) * 256 + (tk & 31) * 8;
#pragma unroll
      for (int nf = 0; nf < NF; ++nf) {
        const int hh = (ncol >> 4) + nf;
        const bool isq = hh < 32, isk = (hh >> 5) == 1;
        const float sc = isq ? 0.25f * 1.4426950408889634f : 1.f;
        u16* base = out + (size_t)hh * (SEQ * 16);
        u32x2 o[MF];
#pragma unroll
        for (int mf = 0; mf < MF; ++mf) {
          const f32x4 y = acc[mf][nf] * (rstd[mf] * sc) + c0[nf] * (nmr[mf] * sc);
          o[mf] = u32x2{cvtpk(y[0], y[1]), cvtpk(y[2], y[3])};
        }
        st16(base + (isk ? kof : lin), pair16(o[0], o[MF - 1]));
      }
    } else {
      u32x2 o[MF][NF];
#pragma unroll
      for (int nf = 0; nf < NF; ++nf) {
        const u32x2 bb = *(const u32x2*)(smem + EOFF + 16384 + ((f0 + nf) * 16 + g * 4) * 2);
        const f32x4 bias = {bflo(bb[0]), bfhi(bb[0]), bflo(bb[1]), bfhi(bb[1])};
#pragma unroll
        for (int mf = 0; mf < MF; ++mf) {
          const f32x4 h = acc[mf][nf] * rstd[mf] + (c0[nf] * nmr[mf] + bias);
          const f32x4 t = h * 1.4426950408889634f;
          f32x4 u, rc;
#pragma unroll
          for (int j = 0; j < 4; ++j) u[j] = __builtin_amdgcn_exp2f(t[j]);
          const f32x4 d = u * (u * -0.5f - 1.f) - 1.f;
#pragma unroll
          for (int j = 0; j < 4; ++j) rc[j] = __builtin_amdgcn_rcpf(d[j]);
          const f32x4 a = h * rc + h;
          o[mf][nf] = u32x2{cvtpk(a[0], a[1]), cvtpk(a[2], a[3])};
          if (nf & 1) st16pair(out + (size_t)(trow + mf * 16) * N + ncol + (nf - 1) * 16, g, o[mf][nf - 1], o[mf][nf]);
        }
      }
      if (NF & 1) st16(out + (size_t)tk * N + ncol + (NF - 1) * 16 + (g >> 1) * 8, pair16(o[0][NF - 1], o[MF - 1][NF - 1]));
    }
  } else {
    float s1 = 0.f, s2 = 0.f;
    u32x2 o[NF];
#pragma unroll
    for (int nf = 0; nf < NF; ++nf) {
      const u32x2 rr = *(const u32x2*)(smem + EOFF + (wm * 16 + l15) * 128 + ((((f0 + nf) * 2 + (g >> 1)) ^ (l15 >> 1)) * 16) + (g & 1) * 8);
      const f32x4 r = acc[0][nf];
      o[nf] = u32x2{cvtpk(r[0] + bflo(rr[0]), r[1] + bfhi(rr[0])), cvtpk(r[2] + bflo(rr[1]), r[3] + bfhi(rr[1]))};
      const float e0 = bflo(o[nf][0]), e1 = bfhi(o[nf][0]), e2 = bflo(o[nf][1]), e3 = bfhi(o[nf][1]);
      s1 += (e0 + e1) + (e2 + e3);
      s2 += (e0 * e0 + e1 * e1) + (e2 * e2 + e3 * e3);
    }
    st16pair(out + (size_t)trow * N + ncol, g, o[0], o[1]);
    s1 = sum32(sum16(s1)); s2 = sum32(sum16(s2));
    if (g == 0) st8(stats + (trow >> 7) * 4096 + (ncol >> 5) * 256 + (trow & 127) * 2, f32x2{s1, s2});
  }
  if (!FIRST) signal_done(smem, scnt, lane, EPI == EPI_QKV ? 48 : 16);
}

template <int BM, int BN, int KTOT, int N, int NA, int NB, int PB, int SUB, int PD, int EPI, bool FIRST>
DI void gemm_phase(char* smem, const u16* Ap, const u16* Bp, const u16* aux, u16* out, float* stats, int m0, int n0,
                   unsigned* wcnt, unsigned wtarget, unsigned* scnt, unsigned ph, unsigned narr, float* csio, bool csin) {
  asm volatile("" : "+s"(Ap), "+s"(Bp), "+s"(aux), "+s"(out), "+s"(stats), "+s"(m0), "+s"(n0));
  constexpr int NK = KTOT / (64 * SUB);
  constexpr int ASUB = BM * 128, BSUB = BL<BN>::STAGE, ASTG = ASUB * SUB, BSTG = BSUB * SUB, BOFF = NA * ASTG;
  static_assert(BOFF + NB * BSTG <= EOFF, "rings exceed LDS");
  constexpr int LA = BM / 16;
  constexpr int LB = BL<BN>::NI / 2;
  constexpr int LM = LA > LB ? LA : LB;
  constexpr bool LN = (EPI != EPI_RES);
  constexpr bool AHM = (EPI == EPI_RES && KTOT == 512);
  constexpr int NE = (EPI == EPI_RES) ? 2 : (FIRST ? 0 : (EPI == EPI_FFN1 ? 5 : 4));
  using SA = Sched<NK, NA, 2, LA * SUB, NE>;
  using SB = Sched<NK, NB, PB, LB * SUB, 0>;
  const unsigned lds0 = (unsigned)(size_t)(LDSP(char))smem;
  int tid = threadIdx.x;
  asm volatile("" : "+v"(tid));
  const int lane = tid & 63;
  const int wid = __builtin_amdgcn_readfirstlane(tid >> 6);

  if (wid < 8) {
    if (!FIRST && wid == 0) { wait_cnt(wcnt, wtarget, lane, 1); *SW(8) = ph; }
    constexpr int NF0 = (BN / 16 + 1) / 2, NF1 = BN / 16 - NF0;
    if (NF0 == NF1 || wid < 4) gemm_compute<BM, BN, KTOT, N, NA, NB, SUB, PD, EPI, FIRST, NF0>(smem, out, stats, m0, n0, wid & 3, (wid >> 2) * NF0, lane, scnt, csio, csin);
    else gemm_compute<BM, BN, KTOT, N, NA, NB, SUB, PD, EPI, FIRST, NF1>(smem, out, stats, m0, n0, wid & 3, NF0, lane, scnt, csio, csin);
    return;
  }

  float* csb = (float*)(smem + XOFF);
  float* st = csb + 256;
  const int l15 = lane & 15, g = lane >> 4;
  const int w4 = wid - 8, w2 = w4 & 1;
  const bool isA = w4 < 2;
  unsigned off[LM];
#pragma unroll
  for (int i = 0; i < LM; ++i) {
    if (isA) {
      const int row = (w2 * LA + i) * 8 + (lane >> 3), ch = (lane & 7) ^ ((row >> 1) & 7);
      off[i] = AHM ? (unsigned)((ch >> 1) * 65536 + row * 32 + (ch & 1) * 16) : (unsigned)(row * KTOT * 2 + ch * 16);
    } else {
      constexpr int CPR = BN / 8;
      const int row = (w2 * LB + i) * (BL<BN>::PADDED ? 4 : 64 / CPR) + lane / CPR;
      const int ch = BL<BN>::PADDED ? lane % CPR : (lane % CPR) ^ (BL<BN>::sw(row) << 1);
      off[i] = (unsigned)(row * N * 2 + ch * 16);
    }
  }
  const char* abase = (const char*)Ap + (AHM ? (size_t)m0 * 32 : (size_t)m0 * KTOT * 2);
  const char* bbase = (const char*)Bp + (size_t)n0 * 2;
  auto extras = [&]() {
    if (EPI == EPI_RES) {
#pragma unroll
      for (int i = 0; i < 2; ++i) {
        const int row = (w4 * 2 + i) * 8 + (lane >> 3), ch = (lane & 7) ^ ((row >> 1) & 7);
        glds16s((const char*)aux + ((size_t)m0 * N + n0) * 2, (unsigned)(row * N * 2 + ch * 16), lds0 + EOFF + (w4 * 2 + i) * 1024);
      }
    } else if (!FIRST) {
#pragma unroll
      for (int i = 0; i < 4; ++i) glds16s((const char*)stats + (size_t)m0 * 128 + w4 * 256 + i * 4096, g * 1024 + l15 * 16, lds0 + EOFF + (w4 * 4 + i) * 1024);
      if (EPI == EPI_FFN1) glds16m<32>((const char*)aux + n0 * 2, (l15 < BN / 8 ? l15 : 0) * 16, lds0 + EOFF + 16384);
    }
  };
  auto issueA = [&](int s) {
#pragma unroll
    for (int u = 0; u < SUB; ++u)
#pragma unroll
      for (int i = 0; i < LA; ++i)
        glds16s(abase + (size_t)(s * SUB + u) * (AHM ? 4 * 65536 : 128), off[i], lds0 + (s % NA) * ASTG + u * ASUB + (w2 * LA + i) * 1024);
    if (s == NK - 1) extras();
  };
  auto issueB = [&](int s) {
#pragma unroll
    for (int u = 0; u < SUB; ++u) {
      const unsigned slot = lds0 + BOFF + (s % NB) * BSTG + u * BSUB;
      const char* src = bbase + (size_t)(s * SUB + u) * 64 * N * 2;
#pragma unroll
      for (int i = 0; i < LB; ++i) {
        if (BL<BN>::PADDED) glds16m<BL<BN>::PADDED ? 4 * (BN / 8) : 64>(src, off[i], slot + (w2 * LB + i) * BL<BN>::GS);
        else glds16(src, off[i], slot + (w2 * LB + i) * 1024);
      }
    }
  };
  f32x2 csv = {0.f, 0.f};
  if (LN && csin && w4 == 3 && lane < BN / 2) csv = ld8s(csio + n0 + lane * 2);
  if (isA) {
    if (!FIRST) lds_wait(smem, 8, ph);
#pragma unroll
    for (int s = 0; s < SA::up(0); ++s) issueA(s);
  } else {
    if (!FIRST) lds_wait(smem, 0, narr);
#pragma unroll
    for (int s = 0; s < SB::up(0); ++s) issueB(s);
  }
  const bf16x8 ONES = __builtin_bit_cast(bf16x8, u32x4{0x3f803f80u, 0x3f803f80u, 0x3f803f80u, 0x3f803f80u});
  const unsigned c_rd0 = lds0 + BOFF + BL<BN>::frag(w4, lane), c_rd1 = lds0 + BOFF + BL<BN>::frag(w4 + 4, lane);
  const unsigned a_rd = lds0 + (w4 * 32 + l15) * 128 + ((g ^ (l15 >> 1)) * 16);
  f32x4 cs0 = {0.f, 0.f, 0.f, 0.f}, cs1 = cs0, rs[2] = {cs0, cs0}, rq[2] = {cs0, cs0};
#pragma unroll
  for (int j = 0; j < NK; ++j) {
    if (isA) {
      wait_bar_n(SA::behind(j));
#pragma unroll
      for (int s = SA::up(j); s < SA::up(j + 1); ++s) issueA(s);
    } else {
      wait_bar_n(SB::behind(j) + (j == NK - 1 ? NE : 0));
#pragma unroll
      for (int s = SB::up(j); s < SB::up(j + 1); ++s) issueB(s);
      if (j == NK - 2) extras();
    }
    if (LN && !csin) {
#pragma unroll
      for (int ks = 0; ks < 2; ++ks) {
        const unsigned o = (j % NB) * BSTG + ks * 8 * BL<BN>::GS;
        cs0 = MFMA16(CAT8(trread(c_rd0 + o), trread(c_rd0 + o + BL<BN>::GS)), ONES, cs0);
        cs1 = MFMA16(CAT8(trread(c_rd1 + o), trread(c_rd1 + o + BL<BN>::GS)), ONES, cs1);
        if (FIRST) {
#pragma unroll
          for (int mf = 0; mf < 2; ++mf) {
            const bf16x8 a = ldsread16((a_rd ^ (ks * 64)) + (j % NA) * ASTG + mf * 2048);
            rs[mf] = MFMA16(ONES, a, rs[mf]);
            rq[mf] = MFMA16(a, a, rq[mf]);
          }
        }
      }
      PIN(cs0); PIN(cs1);
      if (FIRST) { PIN(rs[0]); PIN(rs[1]); PIN(rq[0]); PIN(rq[1]); }
    }
  }
  if (LN) {
    if (csin) {
      if (w4 == 3 && lane < BN / 2) *(f32x2*)(csb + lane * 2) = csv;
    } else if (l15 == 0) {
      *(f32x4*)(csb + w4 * 16 + g * 4) = cs0;
      *(f32x4*)(csb + 64 + w4 * 16 + g * 4) = cs1;
    }
    if (FIRST) {
#pragma unroll
      for (int mf = 0; mf < 2; ++mf) {
        const int jj = l15 & 3;
        const float d = (jj == 0) ? rq[mf][0] : (jj == 1) ? rq[mf][1] : (jj == 2) ? rq[mf][2] : rq[mf][3];
        if (g == (l15 >> 2)) *(f32x2*)(st + (w4 * 32 + mf * 16 + l15) * 2) = f32x2{rs[mf][0], d};
      }
    } else {
      wait_vm<0>();
      f32x4 v = *(const f32x4*)(smem + EOFF + (w4 * 4) * 1024 + lane * 16);
#pragma unroll
      for (int i = 1; i < 4; ++i) v += *(const f32x4*)(smem + EOFF + (w4 * 4 + i) * 1024 + lane * 16);
#pragma unroll
      for (int i = 0; i < 4; ++i) v[i] = sum32(sum16(v[i]));
      if (g == 0) *(f32x4*)(st + (w4 * 32 + 2 * l15) * 2) = v;
    }
  }
  wait_bar<0>();
}

constexpr int AT_K = 0, AT_V = 65536, AT_ONE = 131072, AT_Q = AT_ONE + 6144, AT_PF = AT_Q + 8 * 9 * 256;
static_assert(AT_PF + 1024 <= XOFF, "attention LDS");

#define ATT_HALF(SC, OO, VC, MID)                                                                            \
  {                                                                                                          \
    _Pragma("unroll") for (int i = 0; i < 4; ++i) SC[i] = __builtin_amdgcn_exp2f(SC[i]);                     \
    SBAR(); MID; SBAR();                                                                                     \
    _Pragma("unroll") for (int i = 4; i < 9; ++i) SC[i] = __builtin_amdgcn_exp2f(SC[i]);                     \
    u32x4 p0 = {cvtpk(SC[0], SC[1]), cvtpk(SC[2], SC[3]), cvtpk(SC[4], SC[5]), cvtpk(SC[6], SC[7])};         \
    PIN(p0); SBAR();                                                                                         \
    SC[9] = __builtin_amdgcn_exp2f(SC[9]); SBAR();                                                           \
    OO = MFMA32(CAT8(VC[0], VC[1]), __builtin_bit_cast(bf16x8, p0), OO); SBAR();                             \
    _Pragma("unroll") for (int i = 10; i < 16; ++i) SC[i] = __builtin_amdgcn_exp2f(SC[i]);                   \
    u32x4 p1 = {cvtpk(SC[8], SC[9]), cvtpk(SC[10], SC[11]), cvtpk(SC[12], SC[13]), cvtpk(SC[14], SC[15])};   \
    OO = MFMA32(CAT8(VC[2], VC[3]), __builtin_bit_cast(bf16x8, p1), OO); SBAR();                             \
  }
#define ATT_TILE(VC, VN, T)                                                                                  \
  ATT_HALF(s0, o0, VC, { s1 = MFMA32(kf, qf1, cm1); PIN(s1); kf = ldsread16(kaddr + ((T) + 1) * 1024);       \
       VN[0] = trread(vaddr + ((T) + 1) * 1024); VN[1] = trread(vaddr + ((T) + 1) * 1024 + 256); })          \
  ATT_HALF(s1, o1, VC, { s0 = MFMA32(kf, qf0, cm0); PIN(s0);                                                 \
       VN[2] = trread(vaddr + ((T) + 1) * 1024 + 512); VN[3] = trread(vaddr + ((T) + 1) * 1024 + 768); })

template <bool FAST>
DI void attn_pass(unsigned lds0, int lane, int ks, const bf16x8 qf0, const bf16x8 qf1, float nm0, float nm1, f32x16& o0, f32x16& o1) {
  const int h = lane >> 5;
  unsigned kaddr = lds0 + AT_K + ks * 32768 + h * 512 + (lane & 31) * 16;
  const bool cl = (lane >> 4) & 1;
  const int qq = (lane & 15) >> 2, pp = lane & 3;
  unsigned vaddr = cl ? lds0 + AT_ONE + (4 * (h ^ 1) + qq) * 32 + pp * 8 : lds0 + AT_V + ks * 32768 + (4 * h + qq) * 32 + pp * 8;
  const unsigned vstep = cl ? 0u : 4096u;
  f32x16 cm0, cm1, s0, s1;
  s16x4 vA[4], vB[4];
#pragma unroll
  for (int i = 0; i < 16; ++i) { cm0[i] = FAST ? 0.f : nm0; cm1[i] = FAST ? 0.f : nm1; o0[i] = 0.f; o1[i] = 0.f; }
  bf16x8 kf = ldsread16(kaddr);
  s0 = MFMA32(kf, qf0, cm0);
  vA[0] = trread(vaddr); vA[1] = trread(vaddr + 256); vA[2] = trread(vaddr + 512); vA[3] = trread(vaddr + 768);
  for (int it = 0; it < 8; ++it) {
    PIN(kaddr); PIN(vaddr);
    ATT_TILE(vA, vB, 0) ATT_TILE(vB, vA, 1) ATT_TILE(vA, vB, 2) ATT_TILE(vB, vA, 3)
    kaddr += 4096; vaddr += vstep;
    if (it == 0) lds_bar();
  }
}

DI void attn_first(unsigned lds0, const u16* QKV, int w4, int lane) {
  const int bid = blockIdx.x, head = (bid & 7) * 4 + ((bid >> 3) & 3), qb = bid >> 5;
  const unsigned l16 = lane * 16;
  const char* qsrc = (const char*)QKV + ((size_t)head * SEQ + qb * 256) * 32 + w4 * 2048;
  const char* src = (const char*)QKV + (size_t)(32 + 32 * (w4 >> 1) + head) * 65536 + (w4 & 1) * 32768;
  const unsigned dst = lds0 + (w4 >> 1) * 65536 + (w4 & 1) * 32768;
  glds16s(qsrc, l16, lds0 + AT_Q + w4 * 2048); glds16s(qsrc + 1024, l16, lds0 + AT_Q + w4 * 2048 + 1024);
#pragma unroll
  for (int i = 0; i < 8; ++i) glds16s(src + i * 1024, l16, dst + i * 1024);
}

DI void attn_phase(char* smem, const u16* QKV, u16* O, const u16* wo, const u16* w1, const u16* w2, unsigned* cq, bool wait, unsigned* scnt) {
  asm volatile("" : "+s"(QKV), "+s"(O));
  const unsigned lds0 = (unsigned)(size_t)(LDSP(char))smem;
  int tid = threadIdx.x;
  asm volatile("" : "+v"(tid));
  const int lane = tid & 63;
  const int wid = __builtin_amdgcn_readfirstlane(tid >> 6);
  const int bid = blockIdx.x;
  const int head = (bid & 7) * 4 + ((bid >> 3) & 3), qb = bid >> 5;
  if (wid >= 8) {
    const int w4 = wid - 8;
    const unsigned l16 = lane * 16;
#pragma unroll
    for (int i = 0; i < 2; ++i) { const int t = (w4 * 2 + i) * 64 + lane; if (t < 384) *(u32x4*)(smem + AT_ONE + t * 16) = u32x4{(t & 1) ? 0u : 0x3f80u, 0u, 0u, 0u}; }
    if (tid == 767) *SW(4) = 0u;
    if (wait) lds_wait(smem, 8, 5u);
    const char* src = (const char*)QKV + (size_t)(32 + 32 * (w4 >> 1) + head) * 65536 + (w4 & 1) * 32768;
    const unsigned dst = lds0 + (w4 >> 1) * 65536 + (w4 & 1) * 32768;
    if (wait || !EARLYK) attn_first(lds0, QKV, w4, lane);
    wait_bar<0>();
#pragma unroll
    for (int i = 8; i < 32; ++i) glds16s(src + i * 1024, l16, dst + i * 1024);
    wait_bar<0>();
    {
      const int u = (bid >> 3) * 4 + w4;
      const char* p1 = (u < 28) ? (const char*)wo + u * 18432 : (const char*)w1 + (u - 28) * 18432;
      const int n1 = (u < 28) ? (u == 27 ? 26624 : 18432) : (u == 127 ? 10240 : 18432);
      const char* p2 = (const char*)w2 + (size_t)(bid * 4 + w4) * 1792;
      glds16(p2, l16, lds0 + AT_PF); glds16m<48>(p2 + 1024, l16, lds0 + AT_PF);
      for (int off = 0; off < n1; off += 1024) {
        glds16(p1 + off, l16, lds0 + AT_PF);
        if ((off & 3072) == 3072) __builtin_amdgcn_s_sleep(40);
      }
      wait_vm<0>();
    }
    lds_bar();
    if (__builtin_amdgcn_readfirstlane(*SW(4)) != 0u) { lds_bar(); lds_bar(); }
    return;
  }
  const int wq = wid & 3, ks = wid >> 2;
  if (wait && wid == 0) {
    const int hh = (lane < 3 ? lane : 0) * 32 + head;
    wait_cnt(cq + ((hh / 6) * 64 + (hh % 6) * 8 + qb) * 32, 16, lane, 3);
    *SW(8) = 5u;
  }
  lds_bar();
  const bf16x8 qf0 = ldsread16(lds0 + AT_Q + wq * 2048 + (lane & 31) * 32 + (lane >> 5) * 16);
  const bf16x8 qf1 = ldsread16(lds0 + AT_Q + wq * 2048 + 1024 + (lane & 31) * 32 + (lane >> 5) * 16);
  f32x16 o0, o1;
  attn_pass<true>(lds0, lane, ks, qf0, qf1, 0.f, 0.f, o0, o1);
  float* xb = (float*)(smem + AT_Q);
  if (lane < 32 && !(o0[8] >= 8.67e-19f && o0[8] <= 1.26e30f && o1[8] >= 8.67e-19f && o1[8] <= 1.26e30f)) *SW(4) = 1u;
#pragma unroll
  for (int i = 0; i < 9; ++i) xb[(wid * 9 + i) * 64 + lane] = ks ? o0[i] : o1[i];
  lds_bar();
  if (__builtin_amdgcn_readfirstlane(*SW(4)) != 0u) {
    float m0 = -3.0e38f, m1 = -3.0e38f;
    const unsigned kaddr = lds0 + AT_K + (lane >> 5) * 512 + (lane & 31) * 16;
    f32x16 z;
#pragma unroll
    for (int i = 0; i < 16; ++i) z[i] = 0.f;
    for (int t = 0; t < 64; ++t) {
      const bf16x8 kf = ldsread16(kaddr + t * 1024);
      const f32x16 sa = MFMA32(kf, qf0, z), sb = MFMA32(kf, qf1, z);
#pragma unroll
      for (int i = 0; i < 16; ++i) { m0 = fmaxf(m0, sa[i]); m1 = fmaxf(m1, sb[i]); }
    }
    m0 = max32(m0); m1 = max32(m1);
    attn_pass<false>(lds0, lane, ks, qf0, qf1, -m0, -m1, o0, o1);
#pragma unroll
    for (int i = 0; i < 9; ++i) xb[(wid * 9 + i) * 64 + lane] = ks ? o0[i] : o1[i];
    lds_bar();
  }
  f32x16 o = ks ? o1 : o0;
#pragma unroll
  for (int i = 0; i < 9; ++i) o[i] += xb[((wid ^ 4) * 9 + i) * 64 + lane];
  auto lr = __builtin_amdgcn_permlane32_swap(__float_as_uint(o[8]), __float_as_uint(o[8]), false, false);
  const float rl = __builtin_amdgcn_rcpf(__uint_as_float(lr[0]));
  unsigned x0 = cvtpk(o[0] * rl, o[1] * rl), x1 = cvtpk(o[2] * rl, o[3] * rl);
  unsigned y0 = cvtpk(o[4] * rl, o[5] * rl), y1 = cvtpk(o[6] * rl, o[7] * rl);
  auto r0 = __builtin_amdgcn_permlane32_swap(x0, y0, false, false);
  auto r1 = __builtin_amdgcn_permlane32_swap(x1, y1, false, false);
  const int q0 = qb * 256 + wq * 64 + ks * 32;
  st16(O + ((size_t)head * SEQ + q0 + (lane & 31)) * 16 + (lane >> 5) * 8, u32x4{r0[0], r1[0], r0[1], r1[1]});
  signal_done(smem, scnt, lane, 32);
}

constexpr size_t MB = 1 << 20;
constexpr size_t WS_QKV = 0, WS_O = 12 * MB, WS_X1 = 16 * MB, WS_X2 = 20 * MB, WS_H = 22 * MB, WS_ST = 38 * MB, WS_CNT = 39 * MB, WS_CS = 40 * MB;
constexpr int C_A = 0, C_1 = 256, C_2 = 512, C_3 = 768, C_LAYER = 1024, C_Q = 2048, C_TOTAL = 3072;
constexpr int NF1T = FF / F1BN;

struct Params {
  const u16 *x, *wqkv, *wo, *w1, *b1, *w2;
  u16* out;
  char* ws;
};

extern "C" __global__ __launch_bounds__(768, 3) void k_mega(Params p) {
  __shared__ __attribute__((aligned(256))) char smem[LDS_BYTES];
  if (threadIdx.x == 0) { *SW(0) = 0u; *SW(8) = 0u; }
  if (__builtin_amdgcn_readfirstlane(threadIdx.x) >= 512) {
    __builtin_amdgcn_s_setprio(3);
    if (EARLYK) attn_first((unsigned)(size_t)(LDSP(char))smem, (const u16*)(p.ws + WS_QKV), __builtin_amdgcn_readfirstlane(threadIdx.x >> 6) - 8, threadIdx.x & 63);
  }
  const int bid = blockIdx.x, idx = bid >> 3;
  const int team = (bid & 7) * 2 + (idx >> 4), mem = idx & 15;
  unsigned narr = 0;
  for (int layer = 0; layer < 2; ++layer) {
    asm volatile("" : "+s"(p.ws));
    unsigned* cnt = (unsigned*)(p.ws + WS_CNT);
    unsigned* cl = cnt + layer * C_LAYER * 32;
    u16* O = (u16*)(p.ws + WS_O + layer * 2 * MB);
    u16* X1 = (u16*)(p.ws + WS_X1 + layer * 2 * MB);
    u16* X2 = (u16*)(p.ws + WS_X2);
    u16* H = (u16*)(p.ws + WS_H + layer * 8 * MB);
    float* st1 = (float*)(p.ws + WS_ST + layer * 2 * 262144);
    float* st2 = st1 + 65536;
    float* cs = (float*)(p.ws + WS_CS);
    const int me = team * 16 + mem;
    attn_phase(smem, (const u16*)(p.ws + WS_QKV + layer * 6 * MB), O, p.wo, p.w1, p.w2, cnt + C_Q * 32, layer != 0, cl + (C_A + (bid >> 5) * 32) * 32);
    narr += 8;
    gemm_phase<64, 64, 512, 512, 4, 4, 3, 2, 2, EPI_RES, false>(smem, O, p.wo, layer ? X2 : p.x, X1, st1, team * 128 + (mem >> 3) * 64, (mem & 7) * 64,
                                                                 cl + (C_A + me) * 32, 32, cl + (C_1 + team * 16) * 32, 1 + layer * 8, narr, nullptr, false);
    narr += 8;
    if (mem < NF1T) {
      gemm_phase<128, F1BN, 512, 1792, 3, 5, 3, 1, 2, EPI_FFN1, false>(smem, X1, p.w1, p.b1, H, st1, team * 128, mem * F1BN,
                                                                       cl + (C_1 + me) * 32, 16, cl + (C_2 + team * 16) * 32, 2 + layer * 8, narr, cs + 2048, layer != 0);
      narr += 8;
    }
    gemm_phase<64, 64, 1792, 512, 3, 5, 3, 2, 2, EPI_RES, false>(smem, H, p.w2, X1, layer ? p.out : X2, st2, team * 128 + (mem >> 3) * 64, (mem & 7) * 64,
                                                                  cl + (C_2 + me) * 32, NF1T, cl + (C_3 + team * 16) * 32, 3 + layer * 8, narr, nullptr, false);
    narr += 8;
    if (layer == 0) {
      gemm_phase<128, 96, 512, 1536, 3, 7, 4, 1, 2, EPI_QKV, false>(smem, X2, p.wqkv, nullptr, (u16*)(p.ws + WS_QKV + 6 * MB), st2, team * 128, mem * 96,
                                                                    cl + (C_3 + me) * 32, 16, cnt + (C_Q + mem * 64) * 32, 4, narr, cs, true);
      narr += 8;
    }
  }
}
extern "C" __global__ void k_zend() {}

extern "C" __global__ __launch_bounds__(768, 3) void k_qkv1(Params p) {
  __shared__ __attribute__((aligned(256))) char smem[LDS_BYTES];
  const int bid = blockIdx.x, xcd = bid & 7, idx = bid >> 3;
  if (__builtin_amdgcn_readfirstlane(threadIdx.x) >= 512) __builtin_amdgcn_s_setprio(3);
  if (threadIdx.x < C_TOTAL / 256) *(unsigned*)(p.ws + WS_CNT + (bid * (C_TOTAL / 256) + threadIdx.x) * 128) = 0u;
  unsigned codev = 0;
  {
    unsigned long long lo, hi;
    asm volatile("s_getpc_b64 s[20:21]\n\ts_add_u32 s20, s20, k_mega@rel32@lo+4\n\ts_addc_u32 s21, s21, k_mega@rel32@hi+12\n\ts_mov_b64 %0, s[20:21]\n\t"
                 "s_getpc_b64 s[20:21]\n\ts_add_u32 s20, s20, k_zend@rel32@lo+4\n\ts_addc_u32 s21, s21, k_zend@rel32@hi+12\n\ts_mov_b64 %1, s[20:21]"
                 : "=s"(lo), "=s"(hi)::"s20", "s21", "scc");
    const unsigned long long a = lo + (unsigned long long)(idx * 2048 + threadIdx.x * 4);
    if (threadIdx.x < 512 && a + 4 <= hi) codev = *(const __attribute__((address_space(1))) unsigned*)a;
  }
  gemm_phase<128, 96, 512, 1536, 3, 3, 2, 1, 2, EPI_QKV, true>(smem, p.x, p.wqkv, nullptr, (u16*)(p.ws + WS_QKV), nullptr,
                                                               ((xcd >> 1) * 4 + idx / 8) * 128, ((xcd & 1) * 8 + idx % 8) * 96,
                                                               nullptr, 0, nullptr, 0, 0, (float*)(p.ws + WS_CS), false);
  asm volatile("" ::"v"(codev));
}

extern "C" void kernel_launch(void* const* d_in, const int* in_sizes, int n_in, void* d_out, int out_size, void* d_ws,
                              size_t ws_size, hipStream_t stream) {
  Params p;
  p.x = (const u16*)d_in[0]; p.wqkv = (const u16*)d_in[1]; p.wo = (const u16*)d_in[2];
  p.w1 = (const u16*)d_in[4]; p.b1 = (const u16*)d_in[5]; p.w2 = (const u16*)d_in[6];
  p.out = (u16*)d_out; p.ws = (char*)d_ws;
  k_qkv1<<<256, 768, 0, stream>>>(p);
  k_mega<<<256, 768, 0, stream>>>(p);
}
```
